```python
import torch

def _softmax(x, axis=-1):
    return torch.softmax(x, dim=axis)

def _gelu(x):
    return torch.nn.functional.gelu(x, approximate='tanh')

def _silu(x):
    return torch.nn.functional.silu(x)

def _sigmoid(x):
    return torch.sigmoid(x)

def _layernorm(x, axis=-1, eps=1e-05):
    m = x.mean(dim=axis, keepdim=True)
    v = x.var(dim=axis, keepdim=True, unbiased=False)
    return (x - m) / torch.sqrt(v + eps)

def _scatter_add(tgt, idx, src, axis=0):
    return tgt.clone().index_add_(axis, idx, src)

def _segment_sum(src, seg, n):
    out = torch.zeros((n,) + src.shape[1:], dtype=src.dtype)
    return out.index_add_(0, seg, src)

def _pad(x, pw):
    flat = tuple((v for pair in reversed(pw) for v in pair))
    return torch.nn.functional.pad(x, flat)
kernel_name = 'standalone__mm_dit__diffusion'

def setup_inputs():

    _w = lambda a: (lambda t: t.to(torch.bfloat16) if t.is_floating_point() else t)(torch.as_tensor(a))
    B, Ni, Nt, D = (2, 1024, 32, 768)
    return {'x_img': _w((lambda *s: torch.randn(*s, dtype=torch.bfloat16) * 0.05)(B, Ni, D)), 'x_txt': _w((lambda *s: torch.randn(*s, dtype=torch.bfloat16) * 0.05)(B, Nt, D)), 'c': _w((lambda *s: torch.randn(*s, dtype=torch.bfloat16) * 0.05)(B, D)), 'wqkv_img': _w((lambda *s: torch.randn(*s, dtype=torch.bfloat16) * 0.05)(D, 3 * D)), 'wqkv_txt': _w((lambda *s: torch.randn(*s, dtype=torch.bfloat16) * 0.05)(D, 3 * D)), 'wo_img': _w((lambda *s: torch.randn(*s, dtype=torch.bfloat16) * 0.05)(D, D)), 'wo_txt': _w((lambda *s: torch.randn(*s, dtype=torch.bfloat16) * 0.05)(D, D)), 'wmod_img': _w((lambda *s: torch.randn(*s, dtype=torch.bfloat16) * 0.05)(D, 6 * D)), 'wmod_txt': _w((lambda *s: torch.randn(*s, dtype=torch.bfloat16) * 0.05)(D, 6 * D))}

def reference(x_img, x_txt, c, wqkv_img, wqkv_txt, wo_img, wo_txt, wmod_img, wmod_txt):
    B, Ni, D = x_img.shape
    Nt = x_txt.shape[1]
    mod_i = (_silu(c) @ wmod_img).reshape(B, 6, 1, D)
    mod_t = (_silu(c) @ wmod_txt).reshape(B, 6, 1, D)
    shi, sci, gai = (mod_i[:, 0].clone(), mod_i[:, 1].clone(), mod_i[:, 2].clone())
    sht, sct, gat = (mod_t[:, 0].clone(), mod_t[:, 1].clone(), mod_t[:, 2].clone())
    hi = _layernorm(x_img, axis=-1) * (1 + sci) + shi
    ht = _layernorm(x_txt, axis=-1) * (1 + sct) + sht
    qkvi = hi @ wqkv_img
    qkvt = ht @ wqkv_txt
    qi, ki, vi = (qkvi[..., :D].clone(), qkvi[..., D:2 * D].clone(), qkvi[..., 2 * D:].clone())
    qt, kt, vt = (qkvt[..., :D].clone(), qkvt[..., D:2 * D].clone(), qkvt[..., 2 * D:].clone())
    q = torch.concatenate([qt, qi], axis=1)
    k = torch.concatenate([kt, ki], axis=1)
    v = torch.concatenate([vt, vi], axis=1)
    scores = torch.einsum('bqd,bkd->bqk', q, k) * D ** (-0.5)
    p = _softmax(scores, axis=-1)
    a = torch.einsum('bqk,bkd->bqd', p, v)
    at, ai = (a[:, :Nt].clone(), a[:, Nt:].clone())
    out_i = x_img + gai * (ai @ wo_img)
    out_t = x_txt + gat * (at @ wo_txt)
    return (out_i, out_t)
```

```cpp
#include <hip/hip_runtime.h>
#include <hip/hip_bf16.h>
#include <stdint.h>

typedef short    bf16x8 __attribute__((ext_vector_type(8)));
typedef float    f32x16 __attribute__((ext_vector_type(16)));
typedef float    f32x4  __attribute__((ext_vector_type(4)));
typedef float    f32x2  __attribute__((ext_vector_type(2)));
typedef unsigned u32x4  __attribute__((ext_vector_type(4)));
typedef unsigned u32x2  __attribute__((ext_vector_type(2)));
typedef __bf16   bf16x2_t __attribute__((ext_vector_type(2)));

#define DI __device__ __forceinline__
#define LAS __attribute__((address_space(3)))
#define GAS __attribute__((address_space(1)))
#define SBAR() __builtin_amdgcn_sched_barrier(0)

constexpr int D = 768, NI = 1024, NTX = 32;
constexpr int TPB = 33;
constexpr int NTILE = 66;
constexpr int KS = 48;
constexpr int TILE_B = KS * 1024;
constexpr int KSK = 66;
constexpr int PT_B = KSK * 1024;
constexpr int NCH = 7;
constexpr int MSPLIT = 2;

constexpr size_t WS_FLAG = 0;
constexpr size_t WS_MOD  = 4096;
constexpr size_t WS_XN   = WS_MOD + (size_t)2 * MSPLIT * 2 * 2304 * 4;
constexpr size_t WS_QKV  = WS_XN + (size_t)NTILE * TILE_B;
constexpr size_t WS_P    = WS_QKV + (size_t)3 * NTILE * TILE_B;
constexpr size_t WS_VP   = WS_P + (size_t)NTILE * PT_B;
constexpr size_t WS_ST   = WS_VP + (size_t)48 * PT_B;
constexpr size_t WS_AT   = WS_ST + (size_t)NTILE * NCH * 2 * 32 * 4;
constexpr size_t WS_END  = WS_AT + (size_t)2 * TILE_B;

constexpr float SM_C = 0.03608439182435161f * 1.4426950408889634f;

template <int I> struct IC { static constexpr int v = I; };
template <int B, int E, class F> DI void sfor(F&& f) { if constexpr (B < E) { f(IC<B>{}); sfor<B + 1, E>(f); } }

DI unsigned pk_bf16(float a, float b) { f32x2 v = {a, b}; bf16x2_t r = __builtin_convertvector(v, bf16x2_t); return __builtin_bit_cast(unsigned, r); }
DI float bf_lo(unsigned u) { return __uint_as_float(u << 16); }
DI float bf_hi(unsigned u) { return __uint_as_float(u & 0xffff0000u); }
DI f32x16 mfma(u32x4 a, u32x4 b, f32x16 c) {
  return __builtin_amdgcn_mfma_f32_32x32x16_bf16(__builtin_bit_cast(bf16x8, a), __builtin_bit_cast(bf16x8, b), c, 0, 0, 0);
}
DI float xhalf_max(float v) { auto r = __builtin_amdgcn_permlane32_swap(__float_as_uint(v), __float_as_uint(v), false, false); return fmaxf(__uint_as_float(r[0]), __uint_as_float(r[1])); }
DI float xhalf_sum(float v) { auto r = __builtin_amdgcn_permlane32_swap(__float_as_uint(v), __float_as_uint(v), false, false); return __uint_as_float(r[0]) + __uint_as_float(r[1]); }
DI u32x4 pack8(const f32x16& a, int s) {
  u32x4 r = {pk_bf16(a[8 * s], a[8 * s + 1]), pk_bf16(a[8 * s + 2], a[8 * s + 3]), pk_bf16(a[8 * s + 4], a[8 * s + 5]), pk_bf16(a[8 * s + 6], a[8 * s + 7])};
  return r;
}
DI const char* uni(const char* p) {
  unsigned long long u = (unsigned long long)p;
  unsigned lo = __builtin_amdgcn_readfirstlane((unsigned)u), hi = __builtin_amdgcn_readfirstlane((unsigned)(u >> 32));
  return (const char*)(((unsigned long long)hi << 32) | lo);
}

#define WAIT_VM(n) asm volatile("s_waitcnt vmcnt(%0)" ::"n"(n) : "memory")
#define WAIT_LG0() asm volatile("s_waitcnt lgkmcnt(0)" ::: "memory")
template <int OFF> DI void gld16(u32x4& d, unsigned voff, const char* sb) {
  asm volatile("global_load_dwordx4 %0, %1, %2 offset:%3" : "=v"(d) : "v"(voff), "s"(sb), "i"(OFF) : "memory");
}
template <int OFF> DI void gld16a(u32x4& d, unsigned voff, const char* sb) {
  asm volatile("global_load_dwordx4 %0, %1, %2 offset:%3" : "=a"(d) : "v"(voff), "s"(sb), "i"(OFF) : "memory");
}
template <int OFF> DI void gld8(u32x2& d, unsigned voff, const char* sb) {
  asm volatile("global_load_dwordx2 %0, %1, %2 offset:%3" : "=v"(d) : "v"(voff), "s"(sb), "i"(OFF) : "memory");
}
template <int OFF> DI void gld4(float& d, unsigned voff, const char* sb) {
  asm volatile("global_load_dword %0, %1, %2 offset:%3" : "=v"(d) : "v"(voff), "s"(sb), "i"(OFF) : "memory");
}
template <int OFF> DI void gld16_sc1(u32x4& d, unsigned voff, const char* sb) {
  asm volatile("global_load_dwordx4 %0, %1, %2 offset:%3 sc1" : "=v"(d) : "v"(voff), "s"(sb), "i"(OFF) : "memory");
}
DI void glds5(const unsigned (&v)[5], const char* sb, unsigned lds) {
  asm volatile(
      "s_mov_b32 m0, %6\n\ts_nop 0\n\t"
      "global_load_lds_dwordx4 %0, %5\n\t"
      "global_load_lds_dwordx4 %1, %5 offset:1024\n\t"
      "global_load_lds_dwordx4 %2, %5 offset:2048\n\t"
      "global_load_lds_dwordx4 %3, %5 offset:3072\n\t"
      "s_add_u32 m0, m0, 4096\n\ts_nop 0\n\t"
      "global_load_lds_dwordx4 %4, %5"
      ::"v"(v[0]), "v"(v[1]), "v"(v[2]), "v"(v[3]), "v"(v[4]), "s"(sb), "s"(lds) : "memory", "scc");
}
template <int N> DI void gldsN(unsigned v, const char* sb, unsigned lds) {
  static_assert(N >= 1 && N <= 4, "");
  if constexpr (N == 1) asm volatile("s_mov_b32 m0, %2\n\ts_nop 0\n\tglobal_load_lds_dwordx4 %0, %1" ::"v"(v), "s"(sb), "s"(lds) : "memory");
  if constexpr (N == 2) asm volatile("s_mov_b32 m0, %2\n\ts_nop 0\n\tglobal_load_lds_dwordx4 %0, %1\n\tglobal_load_lds_dwordx4 %0, %1 offset:1024" ::"v"(v), "s"(sb), "s"(lds) : "memory");
  if constexpr (N == 3) asm volatile("s_mov_b32 m0, %2\n\ts_nop 0\n\tglobal_load_lds_dwordx4 %0, %1\n\tglobal_load_lds_dwordx4 %0, %1 offset:1024\n\tglobal_load_lds_dwordx4 %0, %1 offset:2048" ::"v"(v), "s"(sb), "s"(lds) : "memory");
  if constexpr (N == 4) asm volatile("s_mov_b32 m0, %2\n\ts_nop 0\n\tglobal_load_lds_dwordx4 %0, %1\n\tglobal_load_lds_dwordx4 %0, %1 offset:1024\n\tglobal_load_lds_dwordx4 %0, %1 offset:2048\n\tglobal_load_lds_dwordx4 %0, %1 offset:3072" ::"v"(v), "s"(sb), "s"(lds) : "memory");
}
template <int OFF> DI void lds_tr(u32x2& d, unsigned a) { asm volatile("ds_read_b64_tr_b16 %0, %1 offset:%2" : "=v"(d) : "v"(a), "i"(OFF) : "memory"); }
template <int OFF> DI void lds_128(u32x4& d, unsigned a) { asm volatile("ds_read_b128 %0, %1 offset:%2" : "=v"(d) : "v"(a), "i"(OFF) : "memory"); }
template <int OFF> DI void lds_128f(f32x4& d, unsigned a) { asm volatile("ds_read_b128 %0, %1 offset:%2" : "=v"(d) : "v"(a), "i"(OFF) : "memory"); }
template <class T> DI void tie1(T& x) { asm volatile("" : "+v"(x)); }
template <class... T> DI void tie(T&... x) { (tie1(x), ...); }
template <class T> DI void tie1a(T& x) { asm volatile("" : "+a"(x)); }
template <class... T> DI void tiea(T&... x) { (tie1a(x), ...); }

constexpr int K0_MODWG = 2 * 36 * MSPLIT;
constexpr int K0_GRID = K0_MODWG + NTILE;
__global__ __launch_bounds__(256) void k0_prep(const unsigned short* __restrict__ x_img, const unsigned short* __restrict__ x_txt,
                                               const unsigned short* __restrict__ cvec, const unsigned short* __restrict__ wmod_img,
                                               const unsigned short* __restrict__ wmod_txt, char* __restrict__ ws) {
  __shared__ __attribute__((aligned(16))) float sm[4096 + 1024];
  const int tid = threadIdx.x, bid = blockIdx.x;
  if (bid < K0_MODWG) {
    constexpr int ROWS = D / MSPLIT, NLD = ROWS / 32;
    const int type = bid / (36 * MSPLIT), rem = bid % (36 * MSPLIT), slab = rem / MSPLIT, ksp = rem % MSPLIT;
    const unsigned short* W = type ? wmod_txt : wmod_img;
    const int cg = tid & 7, rg = tid >> 3;
    const unsigned short* wp = W + (size_t)(ksp * ROWS + rg) * (6 * D) + slab * 64 + cg * 8;
    u32x4 w[NLD];
#pragma unroll
    for (int i = 0; i < NLD; ++i) w[i] = __builtin_nontemporal_load((const u32x4*)(wp + (size_t)i * 32 * (6 * D)));
    float* sc = sm + 4096;
    for (int i = tid; i < 2 * ROWS; i += 256) {
      const int b = i / ROWS, k = i % ROWS;
      const float cv = __uint_as_float((unsigned)cvec[b * D + ksp * ROWS + k] << 16);
      const float s = cv / (1.f + __expf(-cv));
      sc[i] = __uint_as_float(pk_bf16(s, 0.f) << 16);
    }
    __syncthreads();
    float a0[8], a1[8];
#pragma unroll
    for (int j = 0; j < 8; ++j) { a0[j] = 0.f; a1[j] = 0.f; }
#pragma unroll
    for (int i = 0; i < NLD; ++i) {
      const float s0 = sc[rg + 32 * i], s1 = sc[ROWS + rg + 32 * i];
#pragma unroll
      for (int j = 0; j < 4; ++j) {
        const float lo = bf_lo(w[i][j]), hi = bf_hi(w[i][j]);
        a0[2 * j] = fmaf(s0, lo, a0[2 * j]); a0[2 * j + 1] = fmaf(s0, hi, a0[2 * j + 1]);
        a1[2 * j] = fmaf(s1, lo, a1[2 * j]); a1[2 * j + 1] = fmaf(s1, hi, a1[2 * j + 1]);
      }
    }
    f32x4* rp = (f32x4*)(sm + rg * 128 + cg * 8);
    rp[0] = f32x4{a0[0], a0[1], a0[2], a0[3]}; rp[1] = f32x4{a0[4], a0[5], a0[6], a0[7]};
    rp[16] = f32x4{a1[0], a1[1], a1[2], a1[3]}; rp[17] = f32x4{a1[4], a1[5], a1[6], a1[7]};
    __syncthreads();
    {
      const int o = tid & 127, hf = tid >> 7;
      float s = 0.f;
#pragma unroll
      for (int r = 0; r < 16; ++r) s += sm[(hf * 16 + r) * 128 + o];
      __syncthreads();
      if (hf) sm[o] = s;
      __syncthreads();
      if (!hf) {
        s += sm[o];
        float* mp = (float*)(ws + WS_MOD) + ((size_t)(type * MSPLIT + ksp) * 2 + (o >> 6)) * 2304 + slab * 64 + (o & 63);
        *mp = s;
      }
    }
    if (bid == 0 && tid < 2) *(unsigned*)(ws + WS_FLAG + tid * 256) = 0u;
  } else {
    const int tt = bid - K0_MODWG, b = tt / TPB, i = tt % TPB;
    const int w = tid >> 6, lane = tid & 63, r = lane & 31, h = lane >> 5;
    const unsigned short* xr = (i < 32) ? x_img + ((size_t)b * NI + i * 32 + r) * D : x_txt + ((size_t)b * NTX + r) * D;
    u32x4 v[12];
#pragma unroll
    for (int s = 0; s < 12; ++s) v[s] = *(const u32x4*)(xr + (12 * w + s) * 16 + 8 * h);
    float sum = 0.f;
#pragma unroll
    for (int s = 0; s < 12; ++s)
#pragma unroll
      for (int j = 0; j < 4; ++j) sum += bf_lo(v[s][j]) + bf_hi(v[s][j]);
    sum = xhalf_sum(sum);
    if (h == 0) sm[w * 32 + r] = sum;
    __syncthreads();
    const float mean = (sm[r] + sm[32 + r] + sm[64 + r] + sm[96 + r]) * (1.f / D);
    float sq = 0.f;
#pragma unroll
    for (int s = 0; s < 12; ++s)
#pragma unroll
      for (int j = 0; j < 4; ++j) { const float a = bf_lo(v[s][j]) - mean, c = bf_hi(v[s][j]) - mean; sq = fmaf(a, a, sq); sq = fmaf(c, c, sq); }
    sq = xhalf_sum(sq);
    if (h == 0) sm[128 + w * 32 + r] = sq;
    __syncthreads();
    const float var = (sm[128 + r] + sm[160 + r] + sm[192 + r] + sm[224 + r]) * (1.f / D);
    const float rstd = 1.f / sqrtf(var + 1e-5f);
    char* dst = ws + WS_XN + (size_t)tt * TILE_B + (size_t)(12 * w) * 1024 + lane * 16;
#pragma unroll
    for (int s = 0; s < 12; ++s) {
      u32x4 o;
#pragma unroll
      for (int j = 0; j < 4; ++j) o[j] = pk_bf16((bf_lo(v[s][j]) - mean) * rstd, (bf_hi(v[s][j]) - mean) * rstd);
      *(u32x4*)(dst + s * 1024) = o;
    }
  }
}

constexpr int SLOT_B = 20480, NSLOT = 7, RING_B = SLOT_B * NSLOT;
constexpr int NGRP = 12;
constexpr int LDS_MV = RING_B;
constexpr int LDS_GEMM = RING_B + 12288;

template <int MODE, class Mid>
DI void gemm_core(f32x16 (&acc)[5], const char* shared_base, const unsigned (&voff)[5], unsigned stage_stride,
                  const char* priv_base, unsigned lds0, int wave, int lane, unsigned mv_addr  ,
                  Mid&& mid  ) {
  const unsigned l16 = lane * 16;
  const unsigned ldsw = lds0 + wave * 5120;
  u32x4 xr[NGRP][4];
  auto issue = [&](auto G) {
    constexpr int g = decltype(G)::v;
    glds5(voff, shared_base + (size_t)g * stage_stride, ldsw + (g % NSLOT) * SLOT_B);
    const char* pb = priv_base + g * 4096;
    gld16<0>(xr[g][0], l16, pb); gld16<1024>(xr[g][1], l16, pb); gld16<2048>(xr[g][2], l16, pb); gld16<3072>(xr[g][3], l16, pb);
  };
  const int h = lane >> 5, blk = (lane >> 4) & 1, q4 = (lane & 15) >> 2, p = lane & 3;
  unsigned rb;
  if constexpr (MODE == 0) rb = lds0 + (8 * h + q4) * 320 + blk * 32 + p * 8;
  else if constexpr (MODE == 1) rb = lds0 + (4 * h + q4) * 320 + blk * 32 + p * 8;
  else rb = lds0 + l16;
  const unsigned rb1 = rb + 61440, rb2 = rb + 122880;
  constexpr int TOFF = (MODE == 0) ? 4 * 320 : 8 * 320;
  u32x2 wl[2][5], wh[2][5];
  u32x4 wf[2][5];
  f32x4 mv[2][4];
  auto rd_shared = [&](auto S) {
    constexpr int s = decltype(S)::v, g = s >> 2, j = s & 3, slot = g % NSLOT, bsel = slot / 3, so = (slot % 3) * SLOT_B + j * 5120, cur = s & 1;
    const unsigned a = bsel == 0 ? rb : (bsel == 1 ? rb1 : rb2);
    sfor<0, 5>([&](auto C) {
      constexpr int c = decltype(C)::v;
      if constexpr (MODE == 2) lds_128<so + c * 1024>(wf[cur][c], a);
      else { lds_tr<so + c * 64>(wl[cur][c], a); lds_tr<so + c * 64 + TOFF>(wh[cur][c], a); }
    });
  };
  auto rd_mv = [&](auto S) {
    constexpr int s = decltype(S)::v, cur = s & 1;
    if constexpr (MODE == 0 && s < KS) {
      lds_128f<s * 64>(mv[cur][0], mv_addr); lds_128f<s * 64 + 16>(mv[cur][1], mv_addr);
      lds_128f<3072 + s * 64>(mv[cur][2], mv_addr); lds_128f<3072 + s * 64 + 16>(mv[cur][3], mv_addr);
    }
  };
  auto modulate = [&](auto S) -> u32x4 {
    constexpr int s = decltype(S)::v, cur = s & 1;
    const u32x4 x = xr[s >> 2][s & 3];
    if constexpr (MODE != 0) return x;
    else {
      u32x4 o;
      o[0] = pk_bf16(fmaf(bf_lo(x[0]), mv[cur][0][0], mv[cur][2][0]), fmaf(bf_hi(x[0]), mv[cur][0][1], mv[cur][2][1]));
      o[1] = pk_bf16(fmaf(bf_lo(x[1]), mv[cur][0][2], mv[cur][2][2]), fmaf(bf_hi(x[1]), mv[cur][0][3], mv[cur][2][3]));
      o[2] = pk_bf16(fmaf(bf_lo(x[2]), mv[cur][1][0], mv[cur][3][0]), fmaf(bf_hi(x[2]), mv[cur][1][1], mv[cur][3][1]));
      o[3] = pk_bf16(fmaf(bf_lo(x[3]), mv[cur][1][2], mv[cur][3][2]), fmaf(bf_hi(x[3]), mv[cur][1][3], mv[cur][3][3]));
      return o;
    }
  };
  auto tie_shared = [&](auto S) {
    constexpr int cur = decltype(S)::v & 1;
    if constexpr (MODE == 2) tie(wf[cur][0], wf[cur][1], wf[cur][2], wf[cur][3], wf[cur][4]);
    else tie(wl[cur][0], wl[cur][1], wl[cur][2], wl[cur][3], wl[cur][4], wh[cur][0], wh[cur][1], wh[cur][2], wh[cur][3], wh[cur][4]);
  };
  auto tie_mv = [&](auto S) {
    constexpr int s = decltype(S)::v, cur = s & 1;
    if constexpr (MODE == 0 && s < KS) tie(mv[cur][0], mv[cur][1], mv[cur][2], mv[cur][3]);
  };
  auto tie_grp = [&](auto G) {
    constexpr int g = decltype(G)::v;
    tie(xr[g][0], xr[g][1], xr[g][2], xr[g][3]);
  };

  sfor<0, 6>([&](auto G) { issue(G); });
  if constexpr (MODE == 0) { WAIT_VM(54); mid(); }
  issue(IC<6>{});
  WAIT_VM(54); WAIT_LG0(); tie_grp(IC<0>{});
  __builtin_amdgcn_s_barrier();
  rd_mv(IC<0>{}); rd_mv(IC<1>{});
  if constexpr (MODE == 0) { WAIT_LG0(); tie_mv(IC<0>{}); tie_mv(IC<1>{}); }
  rd_shared(IC<0>{});
  SBAR();
  u32x4 hcur = modulate(IC<0>{});
  SBAR();
  sfor<0, KS>([&](auto S) {
    constexpr int s = decltype(S)::v, g = s >> 2, j = s & 3, cur = s & 1;
    WAIT_LG0(); tie_shared(S); tie_mv(IC<s + 1>{});
    if constexpr (j == 3 && g + 1 < NGRP) {
      constexpr int left = (g + 6 < NGRP ? g + 6 : NGRP - 1) - (g + 1);
      WAIT_VM(9 * left); tie_grp(IC<g + 1>{});
      __builtin_amdgcn_s_barrier();
      if constexpr (g + 7 < NGRP) issue(IC<g + 7>{});
    }
    if constexpr (s + 1 < KS) rd_shared(IC<s + 1>{});
    u32x4 hnext = hcur;
    rd_mv(IC<s + 2>{});
    SBAR();
    sfor<0, 5>([&](auto C) {
      constexpr int c = decltype(C)::v;
      u32x4 wfr;
      if constexpr (MODE == 2) wfr = wf[cur][c];
      else wfr = u32x4{wl[cur][c][0], wl[cur][c][1], wh[cur][c][0], wh[cur][c][1]};
      if constexpr (MODE == 1) acc[c] = mfma(hcur, wfr, acc[c]);
      else acc[c] = mfma(wfr, hcur, acc[c]);
    });
    if constexpr (s + 1 < KS) hnext = modulate(IC<s + 1>{});
    hcur = hnext;
    SBAR();
  });
}

DI void w_voff(unsigned (&voff)[5], int wave, int lane, unsigned pitch) {
#pragma unroll
  for (int i = 0; i < 5; ++i) {
    const unsigned o = i * 1024 + lane * 16, row = o / 320, cb = o % 320;
    voff[i] = (wave * 16 + row) * pitch + cb - (i < 4 ? i * 1024 : 0);
  }
}

__global__ __launch_bounds__(256, 1) void k1_qkv(const char* __restrict__ wqkv_img, const char* __restrict__ wqkv_txt, char* __restrict__ ws) {
  extern __shared__ __attribute__((aligned(16))) char smem[];
  const int bid = blockIdx.x, xcd = bid & 7, idx = bid >> 3;
  const int rowhalf = xcd & 1, colq = xcd >> 1;
  const int wave = __builtin_amdgcn_readfirstlane(threadIdx.x >> 6), lane = threadIdx.x & 63;
  int type, ct, tt, b;
  bool active = true;
  if (colq < 3 || idx < 24) { type = 0; ct = colq * 4 + (idx >> 3); b = rowhalf; tt = rowhalf * TPB + (idx & 7) * 4 + wave; }
  else {
    type = 1; ct = rowhalf * 8 + (idx - 24);
    if (ct >= 15) return;
    b = wave & 1; tt = b * TPB + 32; active = wave < 2;
  }
  const int n0 = ct < 14 ? ct * 160 : 2144;
  const unsigned lds0 = (unsigned)(size_t)(LAS char*)smem;
  const char* W = (type ? wqkv_txt : wqkv_img) + n0 * 2;
  unsigned voff[5];
  w_voff(voff, wave, lane, 4608);

  u32x4 m0[3], m1[3];
  {
    const char* mp = ws + WS_MOD + (size_t)type * MSPLIT * 2 * 2304 * 4;
    unsigned src[3];
#pragma unroll
    for (int i = 0; i < 3; ++i) {
      const int f4 = threadIdx.x + 256 * i, bb = f4 / 384, r = f4 % 384;
      src[i] = (bb * 2304 + (r < 192 ? 768 + r * 4 : (r - 192) * 4)) * 4;
    }
    asm volatile("s_nop 4" ::: "memory");
    gld16<0>(m0[0], src[0], mp); gld16<0>(m0[1], src[1], mp); gld16<0>(m0[2], src[2], mp);
    const char* mp1 = mp + 2 * 2304 * 4;
    gld16<0>(m1[0], src[0], mp1); gld16<0>(m1[1], src[1], mp1); gld16<0>(m1[2], src[2], mp1);
  }
  auto mid = [&]() {
    tie(m0[0], m0[1], m0[2], m1[0], m1[1], m1[2]);
#pragma unroll
    for (int i = 0; i < 3; ++i) {
      const int f4 = threadIdx.x + 256 * i, r = f4 % 384;
      const float one = r < 192 ? 1.f : 0.f;
      f32x4 v = __builtin_bit_cast(f32x4, m0[i]) + __builtin_bit_cast(f32x4, m1[i]) + one;
      *(f32x4*)(smem + LDS_MV + f4 * 16) = v;
    }
  };
  f32x16 acc[5];
#pragma unroll
  for (int c = 0; c < 5; ++c) acc[c] = f32x16{};
  const char* priv = uni(ws + WS_XN + (size_t)tt * TILE_B);
  gemm_core<0>(acc, W, voff, 64 * 4608, priv, lds0, wave, lane, lds0 + LDS_MV + b * 6144 + (lane >> 5) * 32, mid);
  if (!active) return;
#pragma unroll
  for (int c = 0; c < 5; ++c) {
    const int f0 = n0 + c * 32;
    if (ct == 14 && c < 3) continue;
    const int mat = f0 / D, st = (f0 % D) >> 4;
    char* dst = ws + WS_QKV + ((size_t)mat * NTILE + tt) * TILE_B + st * 1024 + lane * 16;
    *(u32x4*)dst = pack8(acc[c], 0);
    *(u32x4*)(dst + 1024) = pack8(acc[c], 1);
  }
}

__global__ __launch_bounds__(256, 1) void k2_sv(const char* __restrict__ wo_img, char* __restrict__ ws) {
  extern __shared__ __attribute__((aligned(16))) char smem[];
  const int bid = blockIdx.x, xcd = bid & 7, idx = bid >> 3;
  const int b = xcd >> 2, sub = xcd & 3, qh = sub & 1, ch = sub >> 1;
  const int wave = __builtin_amdgcn_readfirstlane(threadIdx.x >> 6), lane = threadIdx.x & 63;
  const int nq = qh ? 4 : 5, nc = ch ? 3 : 4, nS = nq * nc;
  const unsigned lds0 = (unsigned)(size_t)(LAS char*)smem;
  f32x16 acc[5];
#pragma unroll
  for (int c = 0; c < 5; ++c) acc[c] = f32x16{};
  unsigned voff[5];
  asm volatile("s_nop 4" ::: "memory");
  if (idx < nS) {
    const int qb = qh * 5 + idx % nq, chunk = ch * 4 + idx / nq;
    const int kt0 = chunk < 6 ? chunk * 5 : 28, own0 = chunk < 6 ? 0 : 2;
    const bool active = qb < 8 || wave == 0;
    const int qt = b * TPB + (qb < 8 ? qb * 4 + wave : 32);
#pragma unroll
    for (int i = 0; i < 5; ++i) voff[i] = i * TILE_B + wave * 1024 + lane * 16 - (i < 4 ? i * 1024 : 0);
    const char* Kb = ws + WS_QKV + ((size_t)NTILE + b * TPB + kt0) * TILE_B;
    const char* priv = uni(ws + WS_QKV + (size_t)qt * TILE_B);
    gemm_core<2>(acc, Kb, voff, 4096, priv, lds0, wave, lane, 0, [] {});
    if (!active) return;
    float m = -3.0e38f;
#pragma unroll
    for (int c = 0; c < 5; ++c)
      if (c >= own0) {
#pragma unroll
        for (int r = 0; r < 16; ++r) m = fmaxf(m, acc[c][r]);
      }
    m = xhalf_max(m);
    const float mc = -m * SM_C;
    float l = 0.f;
    char* dst = ws + WS_P + (size_t)qt * PT_B + (size_t)kt0 * 2048 + lane * 16;
#pragma unroll
    for (int c = 0; c < 5; ++c)
      if (c >= own0) {
#pragma unroll
        for (int r = 0; r < 16; ++r) { const float pv = __builtin_amdgcn_exp2f(fmaf(acc[c][r], SM_C, mc)); acc[c][r] = pv; l += pv; }
        *(u32x4*)(dst + c * 2048) = pack8(acc[c], 0);
        *(u32x4*)(dst + c * 2048 + 1024) = pack8(acc[c], 1);
      }
    l = xhalf_sum(l);
    float* st = (float*)(ws + WS_ST) + ((size_t)qt * NCH + chunk) * 64;
    st[lane] = lane < 32 ? m : l;
  } else {
    const int nV = 27 - nS, t0 = sub == 0 ? 0 : (sub == 1 ? 7 : (sub == 2 ? 18 : 30));
    const int j = idx - nS;
    if (j >= nV) return;
    const int t = t0 + j, kb = t / 5, ct = t % 5;
    const bool active = kb < 8 || wave == 0;
    const int ktl = kb < 8 ? kb * 4 + wave : 32;
    const int n0 = ct < 4 ? ct * 160 : 608;
    w_voff(voff, wave, lane, 1536);
    const char* priv = uni(ws + WS_QKV + ((size_t)2 * NTILE + b * TPB + ktl) * TILE_B);
    gemm_core<1>(acc, wo_img + n0 * 2, voff, 64 * 1536, priv, lds0, wave, lane, 0, [] {});
    if (!active) return;
#pragma unroll
    for (int c = 0; c < 5; ++c) {
      if (ct == 4 && c < 1) continue;
      const int dt = (n0 >> 5) + c;
      char* dst = ws + WS_VP + ((size_t)b * 24 + dt) * PT_B + (size_t)ktl * 2048 + lane * 16;
      *(u32x4*)dst = pack8(acc[c], 0);
      *(u32x4*)(dst + 1024) = pack8(acc[c], 1);
    }
  }
}

constexpr int LDS_K3 = 2 * PT_B + 1024;
DI void chunk_scales(float (&f)[NCH], const float (&mm)[NCH], const float (&ll)[NCH]) {
  float m = mm[0];
#pragma unroll
  for (int c = 1; c < NCH; ++c) m = fmaxf(m, mm[c]);
  float l = 0.f;
#pragma unroll
  for (int c = 0; c < NCH; ++c) { f[c] = __builtin_amdgcn_exp2f((mm[c] - m) * SM_C); l = fmaf(f[c], ll[c], l); }
  const float inv = 1.f / l;
#pragma unroll
  for (int c = 0; c < NCH; ++c) f[c] *= inv;
}

__global__ __launch_bounds__(256, 1) void k3_out(const char* __restrict__ x_img, const char* __restrict__ x_txt, const char* __restrict__ wo_txt,
                                                 char* __restrict__ ws, char* __restrict__ out) {
  extern __shared__ __attribute__((aligned(16))) char smem[];
  const int bid = blockIdx.x, xcd = bid & 7, idx = bid >> 3;
  const int b = xcd >> 2, sub = xcd & 3;
  const int wave = __builtin_amdgcn_readfirstlane(threadIdx.x >> 6), lane = threadIdx.x & 63;
  const int r = lane & 31, h = lane >> 5;
  const unsigned lds0 = (unsigned)(size_t)(LAS char*)smem;
  const unsigned l16 = lane * 16;
  if (idx >= 30) return;
  asm volatile("s_nop 4" ::: "memory");
  if (idx >= 6) {
    const int i = idx - 6, qb = (sub & 1) * 4 + (i & 3), dp = (sub >> 1) * 6 + (i >> 2);
    const int qt = b * TPB + qb * 4 + wave;
    float mm[NCH], ll[NCH];
    const char* stp = uni(ws + WS_ST + (size_t)qt * NCH * 256);
    sfor<0, NCH>([&](auto C) { constexpr int c = decltype(C)::v; gld4<c * 256>(mm[c], r * 4, stp); gld4<c * 256 + 128>(ll[c], r * 4, stp); });
    const char* pb = uni(ws + WS_P + (size_t)qt * PT_B);
    const char* vb = uni(ws + WS_VP + ((size_t)b * 24 + dp * 2) * PT_B + (wave >> 1) * PT_B + (wave & 1) * 3072);
    const unsigned ldsv = lds0 + (wave >> 1) * PT_B + (wave & 1) * 3072;
    u32x4 pf[11][6];
    auto issue = [&](auto G) {
      constexpr int g = decltype(G)::v;
      gldsN<3>(l16, vb + g * 6144, ldsv + g * 6144);
      const char* p0 = pb + g * 6144;
      gld16a<0>(pf[g][0], l16, p0); gld16a<1024>(pf[g][1], l16, p0); gld16a<2048>(pf[g][2], l16, p0); gld16a<3072>(pf[g][3], l16, p0);
      gld16a<0>(pf[g][4], l16, p0 + 4096); gld16a<1024>(pf[g][5], l16, p0 + 4096);
    };
    sfor<0, 5>([&](auto G) { issue(G); });
    WAIT_VM(45);
    tie(mm[0], mm[1], mm[2], mm[3], mm[4], mm[5], mm[6], ll[0], ll[1], ll[2], ll[3], ll[4], ll[5], ll[6]);
    issue(IC<5>{}); issue(IC<6>{});
    float f[NCH];
    chunk_scales(f, mm, ll);
    f32x16 acc[2], tmp[2][2];
    acc[0] = f32x16{}; acc[1] = f32x16{};
    u32x4 vf[2][2];
    const unsigned ra = lds0 + l16, ra1 = ra + PT_B;
    auto rd = [&](auto S) {
      constexpr int s = decltype(S)::v, cur = s & 1;
      if constexpr (s < 64) { lds_128<s * 1024>(vf[cur][0], ra); lds_128<s * 1024>(vf[cur][1], ra1); }
      else { lds_128<(s - 2) * 1024>(vf[cur][0], ra + 2048); lds_128<(s - 2) * 1024>(vf[cur][1], ra1 + 2048); }
    };
    auto tie_g = [&](auto G) { constexpr int g = decltype(G)::v;
      tiea(pf[g][0], pf[g][1], pf[g][2], pf[g][3], pf[g][4], pf[g][5]); };
    WAIT_VM(54); tie_g(IC<0>{});
    __builtin_amdgcn_s_barrier();
    rd(IC<0>{});
    u32x2 xv[8]; u32x4 g0[8], g1[8];
    sfor<0, KSK>([&](auto S) {
      constexpr int s = decltype(S)::v, g = s / 6, j = s % 6, cur = s & 1, c = s / 10, cs = s % 10, tb = c & 1;
      WAIT_LG0();
      tie(vf[cur][0], vf[cur][1]);
      if constexpr (j == 5 && g + 1 < 11) {
        constexpr int last = (g + 6 < 11 ? g + 6 : 10);
        constexpr int extra = (g > 6) ? 24 : 0;
        WAIT_VM(9 * (last - (g + 1)) + extra); tie_g(IC<g + 1>{});
        __builtin_amdgcn_s_barrier();
        if constexpr (g + 7 < 11) issue(IC<g + 7>{});
        if constexpr (g == 6) {
          const char* xb = uni(x_img + (((size_t)b * NI + (qb * 4 + wave) * 32) * D + dp * 64) * 2);
          const unsigned xo = (r * D + 4 * h) * 2;
          sfor<0, 8>([&](auto E) { constexpr int e = decltype(E)::v; gld8<e * 16>(xv[e], xo, xb); });
          const char* gb = uni(ws + WS_MOD + ((size_t)b * 2304 + 1536 + dp * 64) * 4);
          sfor<0, 8>([&](auto E) { constexpr int e = decltype(E)::v; gld16<e * 32>(g0[e], h * 16, gb); });
          const char* gb1 = gb + 2 * 2304 * 4;
          sfor<0, 8>([&](auto E) { constexpr int e = decltype(E)::v; gld16<e * 32>(g1[e], h * 16, gb1); });
        }
      }
      if constexpr (s + 1 < KSK) rd(IC<s + 1>{});
      SBAR();
      if constexpr (cs == 0) { tmp[tb][0] = mfma(vf[cur][0], pf[g][j], f32x16{}); tmp[tb][1] = mfma(vf[cur][1], pf[g][j], f32x16{}); }
      else { tmp[tb][0] = mfma(vf[cur][0], pf[g][j], tmp[tb][0]); tmp[tb][1] = mfma(vf[cur][1], pf[g][j], tmp[tb][1]); }
      if constexpr (c > 0 && cs >= 2 && cs < 6) {
        constexpr int q = cs - 2;
#pragma unroll
        for (int e = 0; e < 8; ++e) { const int rr = (q & 1) * 8 + e; acc[q >> 1][rr] = fmaf(tmp[tb ^ 1][q >> 1][rr], f[c - 1], acc[q >> 1][rr]); }
      }
      SBAR();
    });
    WAIT_VM(0);
    tie(xv[0], xv[1], xv[2], xv[3], xv[4], xv[5], xv[6], xv[7]);
    tie(g0[0], g0[1], g0[2], g0[3], g0[4], g0[5], g0[6], g0[7]);
    tie(g1[0], g1[1], g1[2], g1[3], g1[4], g1[5], g1[6], g1[7]);
    char* ob = out + (((size_t)b * NI + (qb * 4 + wave) * 32 + r) * D + dp * 64 + 4 * h) * 2;
#pragma unroll
    for (int e = 0; e < 8; ++e) {
      const int t = e >> 2, gq = e & 3;
      const f32x4 gt = __builtin_bit_cast(f32x4, g0[e]) + __builtin_bit_cast(f32x4, g1[e]);
      float o[4];
#pragma unroll
      for (int k = 0; k < 4; ++k) o[k] = fmaf(tmp[0][t][4 * gq + k], f[6], acc[t][4 * gq + k]);
      u32x2 ov = {pk_bf16(fmaf(gt[0], o[0], bf_lo(xv[e][0])), fmaf(gt[1], o[1], bf_hi(xv[e][0]))),
                  pk_bf16(fmaf(gt[2], o[2], bf_lo(xv[e][1])), fmaf(gt[3], o[3], bf_hi(xv[e][1])))};
      *(u32x2*)(ob + e * 16) = ov;
    }
  } else {
    const int T = sub * 6 + idx;
    const int qt = b * TPB + 32;
    constexpr int LV = 0, LW = 33 * 2048, LR = LW + 768 * 64;
    float mm[NCH], ll[NCH];
    const char* stp = uni(ws + WS_ST + (size_t)qt * NCH * 256);
    sfor<0, NCH>([&](auto C) { constexpr int c = decltype(C)::v; gld4<c * 256>(mm[c], r * 4, stp); gld4<c * 256 + 128>(ll[c], r * 4, stp); });
    const char* vsrc = uni(ws + WS_QKV + ((size_t)2 * NTILE + b * TPB) * TILE_B + T * 2048);
    for (int kt = wave; kt < 33; kt += 4) gldsN<2>(l16, vsrc + (size_t)kt * TILE_B, lds0 + LV + kt * 2048);
    const int ca = wave, cb = wave < 3 ? wave + 4 : 3;
    u32x4 pa[10], pc[10];
    const char* pA = uni(ws + WS_P + (size_t)qt * PT_B + ca * 10240);
    const char* pC = uni(ws + WS_P + (size_t)qt * PT_B + (cb == 6 ? 56 * 1024 : cb * 10240));
    sfor<0, 10>([&](auto S) { constexpr int s = decltype(S)::v; gld16<(s & 3) * 1024>(pa[s], l16, pA + (s >> 2) * 4096); });
    sfor<0, 10>([&](auto S) { constexpr int s = decltype(S)::v; gld16<(s & 3) * 1024>(pc[s], l16, pC + (s >> 2) * 4096); });
    WAIT_VM(0);
    tie(mm[0], mm[1], mm[2], mm[3], mm[4], mm[5], mm[6], ll[0], ll[1], ll[2], ll[3], ll[4], ll[5], ll[6]);
    tie(pa[0], pa[1], pa[2], pa[3], pa[4], pa[5], pa[6], pa[7], pa[8], pa[9]);
    tie(pc[0], pc[1], pc[2], pc[3], pc[4], pc[5], pc[6], pc[7], pc[8], pc[9]);
    {
      const char* wsrc = uni(wo_txt + (size_t)(192 * wave) * 1536 + T * 64);
      const unsigned wv = (lane >> 2) * 1536 + (lane & 3) * 16;
      for (int i = 0; i < 12; ++i) gldsN<1>(wv, wsrc + (size_t)i * 16 * 1536, lds0 + LW + (192 * wave + 16 * i) * 64);
    }
    __builtin_amdgcn_s_barrier();
    float f[NCH];
    chunk_scales(f, mm, ll);
    float fa = f[0], fc = f[4];
    if (wave == 1) { fa = f[1]; fc = f[5]; }
    if (wave == 2) { fa = f[2]; fc = f[6]; }
    if (wave == 3) { fa = f[3]; fc = 0.f; }
    const int blk = (lane >> 4) & 1, q4 = (lane & 15) >> 2, p = lane & 3;
    const unsigned va = lds0 + LV + blk * 1024 + ((p & 1) * 32 + 4 * h + q4) * 16 + (p >> 1) * 8;
    f32x16 ta = f32x16{}, tc = f32x16{};
    {
      const unsigned a0 = va + ca * 10240, a1 = va + (cb == 6 ? 56 * 1024 : cb * 10240);
      const int skip = cb == 6 ? 4 : 0;
      sfor<0, 10>([&](auto S) {
        constexpr int s = decltype(S)::v, o = (s >> 1) * 2048 + (s & 1) * 256;
        u32x2 lo0, hi0, lo1, hi1;
        lds_tr<o>(lo0, a0); lds_tr<o + 128>(hi0, a0); lds_tr<o>(lo1, a1); lds_tr<o + 128>(hi1, a1);
        WAIT_LG0(); tie(lo0, hi0, lo1, hi1);
        ta = mfma(u32x4{lo0[0], lo0[1], hi0[0], hi0[1]}, pa[s], ta);
        if (s >= skip) tc = mfma(u32x4{lo1[0], lo1[1], hi1[0], hi1[1]}, pc[s], tc);
      });
    }
#pragma unroll
    for (int e = 0; e < 16; ++e) ta[e] = ta[e] * fa + (wave == 3 ? 0.f : tc[e] * fc);
    f32x4* red = (f32x4*)(smem + LR);
    if (wave > 0) {
#pragma unroll
      for (int e = 0; e < 4; ++e) red[((wave - 1) * 4 + e) * 64 + lane] = f32x4{ta[4 * e], ta[4 * e + 1], ta[4 * e + 2], ta[4 * e + 3]};
    }
    WAIT_LG0();
    __builtin_amdgcn_s_barrier();
    GAS unsigned* cnt = (GAS unsigned*)(ws + WS_FLAG + b * 256);
    if (wave == 0) {
#pragma unroll
      for (int w = 0; w < 3; ++w)
#pragma unroll
        for (int e = 0; e < 4; ++e) { const f32x4 v = red[(w * 4 + e) * 64 + lane]; ta[4 * e] += v[0]; ta[4 * e + 1] += v[1]; ta[4 * e + 2] += v[2]; ta[4 * e + 3] += v[3]; }
      const u32x4 o0 = pack8(ta, 0), o1 = pack8(ta, 1);
      char* ab = ws + WS_AT + (size_t)b * TILE_B + T * 2048 + l16;
      asm volatile("global_store_dwordx4 %0, %1, off sc1\n\tglobal_store_dwordx4 %0, %2, off offset:1024 sc1\n\ts_nop 1\n\ts_waitcnt vmcnt(0)"
                   ::"v"(ab), "v"(o0), "v"(o1) : "memory");
      if (lane == 0) __hip_atomic_fetch_add(cnt, 1u, __ATOMIC_RELAXED, __HIP_MEMORY_SCOPE_AGENT);
      for (unsigned spins = 0; spins < (1u << 22); ++spins) {
        if ((unsigned)__builtin_amdgcn_readfirstlane(__hip_atomic_load(cnt, __ATOMIC_RELAXED, __HIP_MEMORY_SCOPE_AGENT)) >= 24u) break;
        __builtin_amdgcn_s_sleep(1);
      }
    }
    asm volatile("s_waitcnt vmcnt(0) lgkmcnt(0)" ::: "memory");
    __builtin_amdgcn_s_barrier();
    u32x4 af[12];
    const char* ab = uni(ws + WS_AT + (size_t)b * TILE_B + wave * 12288);
    sfor<0, 12>([&](auto S) { constexpr int s = decltype(S)::v; gld16_sc1<(s & 3) * 1024>(af[s], l16, ab + (s >> 2) * 4096); });
    u32x2 xv[4]; u32x4 g0[4], g1[4];
    {
      const char* xb = uni(x_txt + ((size_t)b * NTX * D + T * 32) * 2);
      const unsigned xo = (r * D + 4 * h) * 2;
      sfor<0, 4>([&](auto E) { constexpr int e = decltype(E)::v; gld8<e * 16>(xv[e], xo, xb); });
      const char* gb = uni(ws + WS_MOD + ((size_t)(MSPLIT * 2 + b) * 2304 + 1536 + T * 32) * 4);
      sfor<0, 4>([&](auto E) { constexpr int e = decltype(E)::v; gld16<e * 32>(g0[e], h * 16, gb); gld16<e * 32>(g1[e], h * 16, gb + 2 * 2304 * 4); });
    }
    WAIT_VM(0);
    tie(af[0], af[1], af[2], af[3], af[4], af[5], af[6], af[7], af[8], af[9], af[10], af[11]);
    tie(xv[0], xv[1], xv[2], xv[3], g0[0], g0[1], g0[2], g0[3], g1[0], g1[1], g1[2], g1[3]);
    f32x16 tb = f32x16{};
    {
      const unsigned wa = lds0 + LW + (192 * wave + 4 * h + q4) * 64 + blk * 32 + p * 8;
      sfor<0, 12>([&](auto S) {
        constexpr int s = decltype(S)::v;
        u32x2 lo, hi;
        lds_tr<s * 1024>(lo, wa); lds_tr<s * 1024 + 512>(hi, wa);
        WAIT_LG0(); tie(lo, hi);
        tb = mfma(u32x4{lo[0], lo[1], hi[0], hi[1]}, af[s], tb);
      });
    }
    if (wave > 0) {
#pragma unroll
      for (int e = 0; e < 4; ++e) red[((wave - 1) * 4 + e) * 64 + lane] = f32x4{tb[4 * e], tb[4 * e + 1], tb[4 * e + 2], tb[4 * e + 3]};
    }
    WAIT_LG0();
    __builtin_amdgcn_s_barrier();
    if (wave == 0) {
      char* ob = out + ((size_t)2 * NI * D + ((size_t)b * NTX + r) * D + T * 32 + 4 * h) * 2;
#pragma unroll
      for (int e = 0; e < 4; ++e) {
        f32x4 o = {tb[4 * e], tb[4 * e + 1], tb[4 * e + 2], tb[4 * e + 3]};
#pragma unroll
        for (int w = 0; w < 3; ++w) o += red[(w * 4 + e) * 64 + lane];
        const f32x4 gt = __builtin_bit_cast(f32x4, g0[e]) + __builtin_bit_cast(f32x4, g1[e]);
        u32x2 ov = {pk_bf16(fmaf(gt[0], o[0], bf_lo(xv[e][0])), fmaf(gt[1], o[1], bf_hi(xv[e][0]))),
                    pk_bf16(fmaf(gt[2], o[2], bf_lo(xv[e][1])), fmaf(gt[3], o[3], bf_hi(xv[e][1])))};
        *(u32x2*)(ob + e * 16) = ov;
      }
    }
  }
}

extern "C" void kernel_launch(void* const* d_in, const int* in_sizes, int n_in, void* d_out, int out_size, void* d_ws, size_t ws_size,
                              hipStream_t stream) {
  (void)in_sizes; (void)n_in; (void)out_size; (void)ws_size;
  const unsigned short* x_img = (const unsigned short*)d_in[0];
  const unsigned short* x_txt = (const unsigned short*)d_in[1];
  const unsigned short* cvec = (const unsigned short*)d_in[2];
  char* ws = (char*)d_ws;
  (void)hipFuncSetAttribute((const void*)k1_qkv, hipFuncAttributeMaxDynamicSharedMemorySize, LDS_GEMM);
  (void)hipFuncSetAttribute((const void*)k2_sv, hipFuncAttributeMaxDynamicSharedMemorySize, LDS_GEMM);
  (void)hipFuncSetAttribute((const void*)k3_out, hipFuncAttributeMaxDynamicSharedMemorySize, LDS_K3);
  k0_prep<<<K0_GRID, 256, 0, stream>>>(x_img, x_txt, cvec, (const unsigned short*)d_in[7], (const unsigned short*)d_in[8], ws);
  k1_qkv<<<256, 256, LDS_GEMM, stream>>>((const char*)d_in[3], (const char*)d_in[4], ws);
  k2_sv<<<256, 256, LDS_GEMM, stream>>>((const char*)d_in[5], ws);
  k3_out<<<256, 256, LDS_K3, stream>>>((const char*)d_in[0], (const char*)d_in[1], (const char*)d_in[6], ws, (char*)d_out);
}
```

```cpp
#include <hip/hip_runtime.h>
#include <hip/hip_bf16.h>
#include <stdint.h>

typedef short    bf16x8 __attribute__((ext_vector_type(8)));
typedef float    f32x16 __attribute__((ext_vector_type(16)));
typedef float    f32x4  __attribute__((ext_vector_type(4)));
typedef float    f32x2  __attribute__((ext_vector_type(2)));
typedef unsigned u32x4  __attribute__((ext_vector_type(4)));
typedef unsigned u32x2  __attribute__((ext_vector_type(2)));
typedef __bf16   bf16x2_t __attribute__((ext_vector_type(2)));

#define DI __device__ __forceinline__
#define LAS __attribute__((address_space(3)))
#define GAS __attribute__((address_space(1)))
#define SBAR() __builtin_amdgcn_sched_barrier(0)

constexpr int D = 768, NI = 1024, NTX = 32;
constexpr int TPB = 33;
constexpr int NTILE = 66;
constexpr int KS = 48;
constexpr int TILE_B = KS * 1024;
constexpr int KSK = 66;
constexpr int PT_B = KSK * 1024;
constexpr int NCH = 7;
constexpr int MSPLIT = 4;

constexpr size_t WS_FLAG = 0;
constexpr size_t WS_MOD  = 4096;
constexpr size_t WS_XN   = WS_MOD + (size_t)2 * MSPLIT * 2 * 2304 * 4;
constexpr size_t WS_QKV  = WS_XN + (size_t)NTILE * TILE_B;
constexpr size_t WS_P    = WS_QKV + (size_t)3 * NTILE * TILE_B;
constexpr size_t WS_VP   = WS_P + (size_t)NTILE * PT_B;
constexpr size_t WS_ST   = WS_VP + (size_t)48 * PT_B;
constexpr size_t WS_AT   = WS_ST + (size_t)NTILE * NCH * 2 * 32 * 4;
constexpr size_t WS_END  = WS_AT + (size_t)2 * TILE_B;

constexpr float SM_C = 0.03608439182435161f * 1.4426950408889634f;

template <int I> struct IC { static constexpr int v = I; };
template <int B, int E, class F> DI void sfor(F&& f) { if constexpr (B < E) { f(IC<B>{}); sfor<B + 1, E>(f); } }

DI unsigned pk_bf16(float a, float b) { f32x2 v = {a, b}; bf16x2_t r = __builtin_convertvector(v, bf16x2_t); return __builtin_bit_cast(unsigned, r); }
DI float bf_lo(unsigned u) { return __uint_as_float(u << 16); }
DI float bf_hi(unsigned u) { return __uint_as_float(u & 0xffff0000u); }
DI f32x16 mfma(u32x4 a, u32x4 b, f32x16 c) {
  return __builtin_amdgcn_mfma_f32_32x32x16_bf16(__builtin_bit_cast(bf16x8, a), __builtin_bit_cast(bf16x8, b), c, 0, 0, 0);
}
template <bool FIRST> DI void mfma_v(f32x16& c, u32x4 a, u32x4 b) {
  if constexpr (FIRST) asm volatile("v_mfma_f32_32x32x16_bf16 %0, %1, %2, 0" : "=&v"(c) : "v"(a), "v"(b));
  else asm volatile("v_mfma_f32_32x32x16_bf16 %0, %1, %2, %0" : "+v"(c) : "v"(a), "v"(b));
}
DI float xhalf_max(float v) { auto r = __builtin_amdgcn_permlane32_swap(__float_as_uint(v), __float_as_uint(v), false, false); return fmaxf(__uint_as_float(r[0]), __uint_as_float(r[1])); }
DI float xhalf_sum(float v) { auto r = __builtin_amdgcn_permlane32_swap(__float_as_uint(v), __float_as_uint(v), false, false); return __uint_as_float(r[0]) + __uint_as_float(r[1]); }
DI u32x4 pack8(const f32x16& a, int s) {
  u32x4 r = {pk_bf16(a[8 * s], a[8 * s + 1]), pk_bf16(a[8 * s + 2], a[8 * s + 3]), pk_bf16(a[8 * s + 4], a[8 * s + 5]), pk_bf16(a[8 * s + 6], a[8 * s + 7])};
  return r;
}
DI unsigned mod_pair(unsigned x, float s0, float t0, float s1, float t1) {
  unsigned o, a, b;
  asm volatile("v_lshlrev_b32 %1, 16, %3\n\tv_and_b32 %2, 0xffff0000, %3\n\tv_fma_f32 %1, %1, %4, %5\n\tv_fma_f32 %2, %2, %6, %7\n\tv_cvt_pk_bf16_f32 %0, %1, %2"
      : "=v"(o), "=&v"(a), "=&v"(b) : "v"(x), "v"(s0), "v"(t0), "v"(s1), "v"(t1));
  return o;
}
DI const char* uni(const char* p) {
  unsigned long long u = (unsigned long long)p;
  unsigned lo = __builtin_amdgcn_readfirstlane((unsigned)u), hi = __builtin_amdgcn_readfirstlane((unsigned)(u >> 32));
  return (const char*)(((unsigned long long)hi << 32) | lo);
}

#define STAMP(i)
#define STAMP_DECL
#define STAMP_OUT(k)
#define STAMP_ARG
#define STAMP_PASS
#define RT_IN(k)
#define RT_OUT(k)

#define ST_POL "sc1"
DI void gst16(char* p, u32x4 v) { asm volatile("global_store_dwordx4 %0, %1, off " ST_POL "\n\ts_nop 1" ::"v"(p), "v"(v) : "memory"); }
#define WAIT_VM(n) asm volatile("s_waitcnt vmcnt(%0)" ::"n"(n) : "memory")
#define WAIT_LG0() asm volatile("s_waitcnt lgkmcnt(0)" ::: "memory")
template <int OFF> DI void gld16(u32x4& d, unsigned voff, const char* sb) {
  asm volatile("global_load_dwordx4 %0, %1, %2 offset:%3" : "=v"(d) : "v"(voff), "s"(sb), "i"(OFF) : "memory");
}
template <int OFF> DI void gld16a(u32x4& d, unsigned voff, const char* sb) {
  asm volatile("global_load_dwordx4 %0, %1, %2 offset:%3" : "=a"(d) : "v"(voff), "s"(sb), "i"(OFF) : "memory");
}
template <int OFF> DI void gld8(u32x2& d, unsigned voff, const char* sb) {
  asm volatile("global_load_dwordx2 %0, %1, %2 offset:%3" : "=v"(d) : "v"(voff), "s"(sb), "i"(OFF) : "memory");
}
template <int OFF> DI void gld4(float& d, unsigned voff, const char* sb) {
  asm volatile("global_load_dword %0, %1, %2 offset:%3" : "=v"(d) : "v"(voff), "s"(sb), "i"(OFF) : "memory");
}
template <int OFF> DI void touch16(u32x4& junk, unsigned voff, const char* sb) {
  asm volatile("global_load_dwordx4 %0, %1, %2 offset:%3" : "+v"(junk) : "v"(voff), "s"(sb), "i"(OFF) : "memory");
}
DI void touch(unsigned& junk, unsigned voff, const char* sb) {
  asm volatile("global_load_dword %0, %1, %2" : "+v"(junk) : "v"(voff), "s"(sb) : "memory");
}
template <int OFF> DI void gld16_sc1(u32x4& d, unsigned voff, const char* sb) {
  asm volatile("global_load_dwordx4 %0, %1, %2 offset:%3 sc1" : "=v"(d) : "v"(voff), "s"(sb), "i"(OFF) : "memory");
}
DI void glds5(const unsigned (&v)[5], const char* sb, unsigned lds) {
  asm volatile(
      "s_mov_b32 m0, %6\n\ts_nop 0\n\t"
      "global_load_lds_dwordx4 %0, %5\n\t"
      "global_load_lds_dwordx4 %1, %5 offset:1024\n\t"
      "global_load_lds_dwordx4 %2, %5 offset:2048\n\t"
      "global_load_lds_dwordx4 %3, %5 offset:3072\n\t"
      "s_add_u32 m0, m0, 4096\n\ts_nop 0\n\t"
      "global_load_lds_dwordx4 %4, %5"
      ::"v"(v[0]), "v"(v[1]), "v"(v[2]), "v"(v[3]), "v"(v[4]), "s"(sb), "s"(lds) : "memory", "scc");
}
template <int N> DI void gldsN(unsigned v, const char* sb, unsigned lds) {
  static_assert(N >= 1 && N <= 4, "");
  if constexpr (N == 1) asm volatile("s_mov_b32 m0, %2\n\ts_nop 0\n\tglobal_load_lds_dwordx4 %0, %1" ::"v"(v), "s"(sb), "s"(lds) : "memory");
  if constexpr (N == 2) asm volatile("s_mov_b32 m0, %2\n\ts_nop 0\n\tglobal_load_lds_dwordx4 %0, %1\n\tglobal_load_lds_dwordx4 %0, %1 offset:1024" ::"v"(v), "s"(sb), "s"(lds) : "memory");
  if constexpr (N == 3) asm volatile("s_mov_b32 m0, %2\n\ts_nop 0\n\tglobal_load_lds_dwordx4 %0, %1\n\tglobal_load_lds_dwordx4 %0, %1 offset:1024\n\tglobal_load_lds_dwordx4 %0, %1 offset:2048" ::"v"(v), "s"(sb), "s"(lds) : "memory");
  if constexpr (N == 4) asm volatile("s_mov_b32 m0, %2\n\ts_nop 0\n\tglobal_load_lds_dwordx4 %0, %1\n\tglobal_load_lds_dwordx4 %0, %1 offset:1024\n\tglobal_load_lds_dwordx4 %0, %1 offset:2048\n\tglobal_load_lds_dwordx4 %0, %1 offset:3072" ::"v"(v), "s"(sb), "s"(lds) : "memory");
}
template <int OFF> DI void lds_tr(u32x2& d, unsigned a) { asm volatile("ds_read_b64_tr_b16 %0, %1 offset:%2" : "=v"(d) : "v"(a), "i"(OFF) : "memory"); }
template <int O0, int O1> DI void lds_tr2(u32x4& d, unsigned a) {
  u32x2 lo, hi;
  asm volatile("ds_read_b64_tr_b16 %0, %2 offset:%3\n\tds_read_b64_tr_b16 %1, %2 offset:%4" : "=&v"(lo), "=&v"(hi) : "v"(a), "i"(O0), "i"(O1) : "memory");
  d = u32x4{lo[0], lo[1], hi[0], hi[1]};
}
template <int OFF> DI void lds_128(u32x4& d, unsigned a) { asm volatile("ds_read_b128 %0, %1 offset:%2" : "=v"(d) : "v"(a), "i"(OFF) : "memory"); }
template <int OFF> DI void lds_128f(f32x4& d, unsigned a) { asm volatile("ds_read_b128 %0, %1 offset:%2" : "=v"(d) : "v"(a), "i"(OFF) : "memory"); }
template <class T> DI void tie1(T& x) { asm volatile("" : "+v"(x)); }
template <class... T> DI void tie(T&... x) { (tie1(x), ...); }
template <class T> DI void tie1a(T& x) { asm volatile("" : "+a"(x)); }
template <class... T> DI void tiea(T&... x) { (tie1a(x), ...); }

constexpr int K0_MODWG = 2 * 24 * MSPLIT;
constexpr int K0_LNWG = 2 * NTILE;
constexpr int K0_PFWG = 72;
constexpr int K0_GRID = K0_MODWG + K0_LNWG + K0_PFWG;
template <int NT, int ROWS>
DI void mod_slab(const unsigned short* __restrict__ W, const unsigned short* __restrict__ cvec, int row0, int col0, float* __restrict__ out, float* sm) {
  constexpr int RG = NT / 8, NLD = ROWS / RG;
  static_assert(RG % 16 == 0 && ROWS % RG == 0 && ROWS / 2 <= NT, "");
  const int tid = threadIdx.x, cg = tid & 7, rg = tid >> 3;
  unsigned c0 = 0, c1 = 0;
  if (tid < ROWS / 2) { c0 = *(const unsigned*)(cvec + row0 + 2 * tid); c1 = *(const unsigned*)(cvec + D + row0 + 2 * tid); }
  const unsigned short* wp = W + (size_t)(row0 + rg) * (6 * D) + col0 + cg * 8;
  u32x4 w[NLD];
#pragma unroll
  for (int i = 0; i < NLD; ++i) w[i] = __builtin_nontemporal_load((const u32x4*)(wp + (size_t)i * RG * (6 * D)));
  float* sc = sm + RG * 128 + 64;
  if (tid < ROWS / 2) {
    auto silu = [](float v) { return v / (1.f + __expf(-v)); };
    const unsigned s0 = pk_bf16(silu(bf_lo(c0)), silu(bf_hi(c0))), s1 = pk_bf16(silu(bf_lo(c1)), silu(bf_hi(c1)));
    *(f32x2*)(sc + 2 * tid) = f32x2{bf_lo(s0), bf_hi(s0)};
    *(f32x2*)(sc + ROWS + 2 * tid) = f32x2{bf_lo(s1), bf_hi(s1)};
  }
  __syncthreads();
  float a0[8], a1[8];
#pragma unroll
  for (int j = 0; j < 8; ++j) { a0[j] = 0.f; a1[j] = 0.f; }
#pragma unroll
  for (int i = 0; i < NLD; ++i) {
    const float s0 = sc[rg + RG * i], s1 = sc[ROWS + rg + RG * i];
#pragma unroll
    for (int j = 0; j < 4; ++j) {
      const float lo = bf_lo(w[i][j]), hi = bf_hi(w[i][j]);
      a0[2 * j] = fmaf(s0, lo, a0[2 * j]); a0[2 * j + 1] = fmaf(s0, hi, a0[2 * j + 1]);
      a1[2 * j] = fmaf(s1, lo, a1[2 * j]); a1[2 * j + 1] = fmaf(s1, hi, a1[2 * j + 1]);
    }
  }
  f32x4* rp = (f32x4*)(sm + rg * 128 + (rg >= RG / 2 ? 16 : 0) + cg * 8);
  rp[0] = f32x4{a0[0], a0[1], a0[2], a0[3]}; rp[1] = f32x4{a0[4], a0[5], a0[6], a0[7]};
  rp[16] = f32x4{a1[0], a1[1], a1[2], a1[3]}; rp[17] = f32x4{a1[4], a1[5], a1[6], a1[7]};
  __syncthreads();
  if (tid < 256) {
    const int o = tid >> 1, hf = tid & 1;
    float s = 0.f;
#pragma unroll
    for (int r = 0; r < RG / 2; ++r) s += sm[(hf * (RG / 2) + r) * 128 + hf * 16 + o];
    s += __shfl_xor(s, 1);
    if (!hf) out[(o >> 6) * 2304 + (o & 63)] = s;
  }
}
DI float x16_sum(float v) { auto r = __builtin_amdgcn_permlane16_swap(__float_as_uint(v), __float_as_uint(v), false, false); return __uint_as_float(r[0]) + __uint_as_float(r[1]); }
__global__ __launch_bounds__(256) void k0_prep(const unsigned short* __restrict__ x_img, const unsigned short* __restrict__ x_txt,
                                               const unsigned short* __restrict__ cvec, const unsigned short* __restrict__ wmod_img,
                                               const unsigned short* __restrict__ wmod_txt, const char* __restrict__ wqkv_img,
                                               const char* __restrict__ wqkv_txt, char* __restrict__ ws) {
  __shared__ __attribute__((aligned(16))) float sm[4096 + 64 + 384];
  const int tid = threadIdx.x;
  int bid = blockIdx.x;
  RT_IN(0); STAMP_DECL; STAMP(0);
  if (bid < K0_MODWG) {
    const int ksp = bid & 3, type = (bid >> 2) & 1, slab = bid >> 3;
    mod_slab<256, D / MSPLIT>(type ? wmod_txt : wmod_img, cvec, ksp * (D / MSPLIT), slab * 64,
                              (float*)(ws + WS_MOD) + (size_t)(type * MSPLIT + ksp) * 2 * 2304 + slab * 64, sm);
    if (bid == 0 && tid < 2) *(unsigned*)(ws + WS_FLAG + tid * 256) = 0u;
    STAMP(5); WAIT_VM(0); STAMP(6); STAMP_OUT(0);
  } else if (bid >= 192 + K0_LNWG) {
    const int pw = bid - (192 + K0_LNWG);
    const char* src = ((pw & 1) ? wqkv_txt : wqkv_img) + (size_t)(pw >> 1) * 24576 + tid * 16;
    u32x4 v[6];
#pragma unroll
    for (int i = 0; i < 6; ++i) v[i] = *(const u32x4*)(src + i * 4096);
#pragma unroll
    for (int i = 0; i < 6; ++i) asm volatile("" ::"v"(v[i]));
  } else {
    const int ht = bid - 192, tt = ht >> 1, half = ht & 1, b = tt >= TPB, i = tt - b * TPB;
    const int w = tid >> 6, lane = tid & 63, r = lane & 15, h = (lane >> 4) & 1, kk = lane >> 5;
    const unsigned short* xr = (i < 32) ? x_img + ((size_t)b * NI + i * 32 + half * 16 + r) * D : x_txt + ((size_t)b * NTX + half * 16 + r) * D;
    u32x4 v[6];
#pragma unroll
    for (int s = 0; s < 6; ++s) v[s] = *(const u32x4*)(xr + (12 * w + 2 * s + kk) * 16 + 8 * h);
    STAMP(11);
    float sum = 0.f;
#pragma unroll
    for (int s = 0; s < 6; ++s)
#pragma unroll
      for (int j = 0; j < 4; ++j) sum += bf_lo(v[s][j]) + bf_hi(v[s][j]);
    const float mw = xhalf_sum(x16_sum(sum)) * (1.f / 192.f);
    STAMP(12);
    float sq = 0.f;
#pragma unroll
    for (int s = 0; s < 6; ++s)
#pragma unroll
      for (int j = 0; j < 4; ++j) { const float a = bf_lo(v[s][j]) - mw, c = bf_hi(v[s][j]) - mw; sq = fmaf(a, a, sq); sq = fmaf(c, c, sq); }
    sq = xhalf_sum(x16_sum(sq));
    if (lane < 16) *(f32x2*)(sm + (r * 4 + w) * 2) = f32x2{mw, sq};
    __syncthreads();
    const f32x4 p0 = *(const f32x4*)(sm + r * 8), p1 = *(const f32x4*)(sm + r * 8 + 4);
    const float mean = (p0[0] + p0[2] + p1[0] + p1[2]) * 0.25f;
    const float d0 = p0[0] - mean, d1 = p0[2] - mean, d2 = p1[0] - mean, d3 = p1[2] - mean;
    const float var = (p0[1] + p0[3] + p1[1] + p1[3] + 192.f * (d0 * d0 + d1 * d1 + d2 * d2 + d3 * d3)) * (1.f / D);
    const float rstd = 1.f / sqrtf(var + 1e-5f);
    char* dst = ws + WS_XN + (size_t)tt * TILE_B + (size_t)(12 * w + kk) * 1024 + (h * 32 + half * 16 + r) * 16;
#pragma unroll
    for (int s = 0; s < 6; ++s) {
      u32x4 o;
#pragma unroll
      for (int j = 0; j < 4; ++j) o[j] = pk_bf16((bf_lo(v[s][j]) - mean) * rstd, (bf_hi(v[s][j]) - mean) * rstd);
      gst16(dst + s * 2048, o);
    }
    STAMP(13); WAIT_VM(0); STAMP(14); STAMP_OUT(0);
  }
  RT_OUT(0);
}

constexpr int SLOT_B = 10240, NSLOT = 12, RING_B = SLOT_B * NSLOT;
constexpr int NSTG = 24, RAHEAD = 12;
constexpr int LDS_MV = RING_B;
constexpr int LDS_GEMM = RING_B + 12288;
#define BAR() __builtin_amdgcn_s_barrier()
#define WAIT_LG(n) asm volatile("s_waitcnt lgkmcnt(%0)" ::"n"(n) : "memory")
template <class T> DI T sel(bool c, T a, T b) { return c ? a : b; }
constexpr int imin(int a, int b) { return a < b ? a : b; }
constexpr int imax(int a, int b) { return a > b ? a : b; }
constexpr int ld_cnt(int k, int ns) { return k == 0 ? 4 : imin(imin(NSTG, ld_cnt(k - 1, ns) + 2), imax(ns, k - 1 + ns - 1)); }

template <int NS, bool PRE>
DI void loader_shared(const char* base, const unsigned (&voff)[5], unsigned stage_stride, unsigned lds  ) {
  const char* p = base;
  sfor<0, NSTG>([&](auto K) {
    constexpr int k = decltype(K)::v;
    sfor<(k == 0 ? 0 : ld_cnt(k - 1, NS)), ld_cnt(k, NS)>([&](auto G) { glds5(voff, p, lds + (decltype(G)::v % NS) * SLOT_B); p += stage_stride; });
    if constexpr (PRE && k == 0) BAR();
    WAIT_VM(5 * (ld_cnt(k, NS) - k - 1)); BAR();
  });
}

DI void priv_prologue(u32x4 (&xr)[KS], const char* priv, unsigned l16) {
  sfor<0, RAHEAD>([&](auto S) { constexpr int s = decltype(S)::v; gld16<(s & 3) * 1024>(xr[s], l16, priv + (s >> 2) * 4096); });
}
template <int MODE>
DI void consumer(f32x16 (&acc)[5], u32x4 (&xr)[KS], const char* priv, unsigned lds0, int lane, unsigned mva   STAMP_ARG) {
  const int h = lane >> 5, blk = (lane >> 4) & 1, q4 = (lane & 15) >> 2, p = lane & 3;
  const unsigned l16 = lane * 16;
  u32x4 hh[2];
  auto ldx = [&](auto S) { constexpr int s = decltype(S)::v; if constexpr (s < KS) gld16<(s & 3) * 1024>(xr[s], l16, priv + (s >> 2) * 4096); };
  unsigned rw;
  if constexpr (MODE == 0) rw = lds0 + (8 * h + q4) * 320 + blk * 32 + p * 8;
  else if constexpr (MODE == 1) rw = lds0 + (4 * h + q4) * 320 + blk * 32 + p * 8;
  else rw = lds0 + l16;
  unsigned rw1 = rw + 6 * SLOT_B;
  tie(rw, rw1);
  constexpr int TOFF = (MODE == 0) ? 4 * 320 : 8 * 320;
  u32x4 wf[2][5];
  f32x4 mv[2][4];
  auto rdw = [&](auto S, auto C) {
    constexpr int s = decltype(S)::v, c = decltype(C)::v, cur = s & 1, slot = (s >> 1) % NSLOT, so = (slot % 6) * SLOT_B + (s & 1) * 5120;
    if constexpr (s < KS) {
      const unsigned a = slot < 6 ? rw : rw1;
      if constexpr (MODE == 2) lds_128<so + c * 1024>(wf[cur][c], a);
      else lds_tr2<so + c * 64, so + c * 64 + TOFF>(wf[cur][c], a);
    }
  };
  auto rdm = [&](auto S, auto J) {
    constexpr int s = decltype(S)::v, j = decltype(J)::v;
    if constexpr (MODE == 0 && s < KS) lds_128f<(j >> 1) * 3072 + s * 64 + (j & 1) * 16>(mv[s & 1][j], mva);
  };
  float ua[4], ub[4];
  auto modg = [&](auto S, auto G) {
    constexpr int s = decltype(S)::v, cur = s & 1, g = decltype(G)::v;
    if constexpr (MODE == 0 && s < KS) {
      const f32x4 &s0 = mv[cur][0], &s1 = mv[cur][1], &t0 = mv[cur][2], &t1 = mv[cur][3];
      unsigned o;
      if constexpr (g == 0)
        asm volatile("v_lshlrev_b32 %0, 16, %4\n\tv_and_b32 %1, 0xffff0000, %4\n\tv_lshlrev_b32 %2, 16, %5\n\tv_and_b32 %3, 0xffff0000, %5"
                     : "=&v"(ua[0]), "=&v"(ub[0]), "=&v"(ua[1]), "=&v"(ub[1]) : "v"(xr[s][0]), "v"(xr[s][1]));
      if constexpr (g == 1) {
        asm volatile("v_fma_f32 %1, %1, %5, %6\n\tv_fma_f32 %2, %2, %7, %8\n\tv_lshlrev_b32 %3, 16, %4\n\tv_cvt_pk_bf16_f32 %0, %1, %2"
                     : "=v"(o), "+v"(ua[0]), "+v"(ub[0]), "=&v"(ua[2]) : "v"(xr[s][2]), "v"(s0[0]), "v"(t0[0]), "v"(s0[1]), "v"(t0[1]));
        hh[cur][0] = o;
      }
      if constexpr (g == 2) {
        asm volatile("v_fma_f32 %1, %1, %5, %6\n\tv_fma_f32 %2, %2, %7, %8\n\tv_and_b32 %3, 0xffff0000, %4\n\tv_cvt_pk_bf16_f32 %0, %1, %2"
                     : "=v"(o), "+v"(ua[1]), "+v"(ub[1]), "=&v"(ub[2]) : "v"(xr[s][2]), "v"(s0[2]), "v"(t0[2]), "v"(s0[3]), "v"(t0[3]));
        hh[cur][1] = o;
      }
      if constexpr (g == 3)
        asm volatile("v_lshlrev_b32 %2, 16, %4\n\tv_and_b32 %3, 0xffff0000, %4\n\tv_fma_f32 %0, %0, %5, %6\n\tv_fma_f32 %1, %1, %7, %8"
                     : "+v"(ua[2]), "+v"(ub[2]), "=&v"(ua[3]), "=&v"(ub[3]) : "v"(xr[s][3]), "v"(s1[0]), "v"(t1[0]), "v"(s1[1]), "v"(t1[1]));
      if constexpr (g == 4) {
        unsigned o2;
        asm volatile("v_fma_f32 %2, %2, %6, %7\n\tv_fma_f32 %3, %3, %8, %9\n\tv_cvt_pk_bf16_f32 %0, %4, %5\n\tv_cvt_pk_bf16_f32 %1, %2, %3"
                     : "=&v"(o), "=&v"(o2), "+v"(ua[3]), "+v"(ub[3]) : "v"(ua[2]), "v"(ub[2]), "v"(s1[2]), "v"(t1[2]), "v"(s1[3]), "v"(t1[3]));
        hh[cur][2] = o; hh[cur][3] = o2;
      }
    }
  };
  STAMP(3);
  BAR();
  STAMP(4);
  if constexpr (MODE == 0) {
    sfor<0, 4>([&](auto J) { rdm(IC<0>{}, J); });
    rdm(IC<1>{}, IC<0>{}); rdw(IC<0>{}, IC<0>{}); rdm(IC<1>{}, IC<2>{}); rdw(IC<0>{}, IC<1>{}); rdm(IC<1>{}, IC<1>{}); rdw(IC<0>{}, IC<2>{});
    WAIT_VM(RAHEAD - 1); WAIT_LG(9);
    sfor<0, 5>([&](auto G) { modg(IC<0>{}, G); });
    rdm(IC<1>{}, IC<3>{}); rdw(IC<0>{}, IC<3>{}); rdw(IC<0>{}, IC<4>{});
  } else {
    sfor<0, 5>([&](auto C) { rdw(IC<0>{}, C); });
  }
  sfor<0, KS>([&](auto S) {
    constexpr int s = decltype(S)::v, cur = s & 1;
    constexpr bool more = s + 1 < KS, more2 = s + 2 < KS;
    if constexpr ((s & 1) && more) BAR();
    if constexpr (MODE == 0) { if constexpr (more) WAIT_VM(imin(RAHEAD - 2, KS - 2 - s)); }
    else WAIT_VM(imin(RAHEAD - 1, KS - 1 - s));
    sfor<0, 5>([&](auto C) {
      constexpr int c = decltype(C)::v;
      constexpr int PW = MODE == 2 ? 1 : 2;
      constexpr int older_tail = (4 - c) * PW + ((MODE == 0 && more) ? (c < 3 ? 3 - c : 0) : 0);
      constexpr int this_head = c * ((more ? PW : 0) + ((MODE == 0 && more2) ? 1 : 0));
      WAIT_LG(older_tail + this_head);
      const u32x4 b = MODE == 0 ? hh[cur] : xr[s];
      if constexpr (MODE == 1) mfma_v<s == 0>(acc[c], b, wf[cur][c]);
      else mfma_v<s == 0>(acc[c], wf[cur][c], b);
      modg(IC<s + 1>{}, C);
      if constexpr (c < 4) rdm(IC<s + 2>{}, IC<(c == 1 ? 2 : (c == 2 ? 1 : c))>{});
      rdw(IC<s + 1>{}, C);
      if constexpr (c == 2) ldx(IC<s + RAHEAD>{});
    });
  });
  asm volatile("s_nop 15" : "+v"(acc[0]), "+v"(acc[1]), "+v"(acc[2]), "+v"(acc[3]), "+v"(acc[4]));
}

DI void w_voff(unsigned (&voff)[5], int lane, unsigned pitch) {
#pragma unroll
  for (int i = 0; i < 5; ++i) {
    const unsigned o = i * 64 + lane, row = o / 20, cb = (o % 20) * 16;
    voff[i] = row * pitch + cb - (i < 4 ? i * 1024 : 0);
  }
}

constexpr int K1_NW = 10, K1_NH = 6, HD = 3, PF_START = 10;
constexpr int K1_H = K1_NW * SLOT_B;
constexpr int K1_MV = K1_H + K1_NH * 8192;
constexpr int LDS_K1 = K1_MV + 12288;
__global__ __launch_bounds__(960, 4) void k1_qkv(const char* __restrict__ wqkv_img, const char* __restrict__ wqkv_txt, const char* __restrict__ wo_img,
                                                 const char* __restrict__ wo_txt, char* __restrict__ ws) {
  extern __shared__ __attribute__((aligned(16))) char smem[];
  STAMP_DECL; STAMP(0); RT_IN(1);
  const int bid = blockIdx.x, xcd = bid & 7, idx = bid >> 3;
  const int rowhalf = xcd & 1, colq = xcd >> 1;
  int wave = __builtin_amdgcn_readfirstlane(threadIdx.x >> 6);
  const int lane = threadIdx.x & 63, h = lane >> 5;
  const bool txt = (colq == 3) & (idx >= 24);
  const int ct = sel(txt, rowhalf * 8 + (idx - 24), colq * 4 + (idx >> 3));
  const int n0 = sel(ct < 14, ct * 160, 2144);
  const unsigned lds0 = (unsigned)(size_t)(LAS char*)smem;
  const unsigned l16 = lane * 16;
  const int j = wave & 3, u = (wave >> 3) & 1;
  const char* xb = ws + WS_XN + (unsigned)(sel(txt, 32 + u * TPB, rowhalf * TPB + (idx & 7) * 4 + u) * TILE_B + j * 1024);
  const char* W = sel(txt, wqkv_txt, wqkv_img) + n0 * 2;
  const unsigned mr = sel(threadIdx.x >= 384, threadIdx.x - 384, threadIdx.x), mb = sel(txt, threadIdx.x >= 384 ? 1 : 0, rowhalf);
  const char* mp = ws + WS_MOD + (unsigned)(sel(txt, MSPLIT * 2, 0) * 2304 * 4);
  asm volatile("s_nop 4" ::"s"(xb), "s"(W), "s"(mp) : "memory");
  if (ct >= 15) return;
  if (__builtin_expect(wave >= 12, 1)) {
    if (wave == 14) {
      u32x4 junk = {0, 0, 0, 0};
      const char* wo = sel((bool)(bid & 1), wo_txt, wo_img) + (bid >> 1) * 9216;
#pragma unroll 1
      for (int i = 0; i < PF_START; ++i) BAR();
#pragma unroll 1
      for (int i = 0; i < 9; ++i) { touch16<0>(junk, l16, wo); wo += 1024; BAR(); }
#pragma unroll 1
      for (int i = 0; i < NSTG + 1 - 9 - PF_START; ++i) BAR();
      WAIT_VM(0); tie(junk);
      return;
    }
    unsigned voff[5];
    w_voff(voff, lane, 4608);
    loader_shared<K1_NW, true>(W + (wave - 12) * 16 * 4608, voff, 32 * 4608, lds0 + (wave - 12) * 5120);
    return;
  }
  u32x4 m[MSPLIT];
  const bool mod_on = txt | (threadIdx.x < 384);
  auto ldm = [&]() {
    const unsigned src = (mb * 576 + (mr < 192 ? 192 + mr : mr - 192)) * 16;
    if (mod_on) sfor<0, MSPLIT>([&](auto K) { constexpr int k = decltype(K)::v; gld16<0>(m[k], src, mp + k * 18432); });
  };
  auto stm = [&]() {
    tie(m[0], m[1], m[2], m[3]);
    auto f = [](u32x4 v) { return __builtin_bit_cast(f32x4, v); };
    if (mod_on) *(f32x4*)(smem + K1_MV + mb * 6144 + mr * 16) = (f(m[0]) + f(m[1])) + (f(m[2]) + f(m[3])) + (mr < 192 ? 1.f : 0.f);
    WAIT_LG(0);
    BAR();
  };
  if (wave >= 4) {
    const unsigned x2 = l16 + sel(txt, 0, 2 * TILE_B);
    u32x4 xr[12][2];
    auto ldx = [&](auto I, auto T) { constexpr int i = decltype(I)::v, t = decltype(T)::v; if constexpr (i < 12) gld16<0>(xr[i][t], t ? x2 : l16, xb + i * 4096); };
    ldm();
    sfor<0, HD>([&](auto I) { ldx(I, IC<0>{}); ldx(I, IC<1>{}); });
    WAIT_VM(2 * HD);
    stm();
    const unsigned mva = lds0 + K1_MV + sel(txt, u, rowhalf) * 6144 + h * 32 + j * 64;
    char* hw = smem + K1_H + ((j >> 1) * 2 + (j & 1)) * 4096 + u * 1024 + l16;
    f32x4 mv[2][4];
    auto rdm = [&](auto I) {
      constexpr int i = decltype(I)::v;
      if constexpr (i < 12) { lds_128f<i * 256>(mv[i & 1][0], mva); lds_128f<i * 256 + 16>(mv[i & 1][1], mva); lds_128f<3072 + i * 256>(mv[i & 1][2], mva); lds_128f<3072 + i * 256 + 16>(mv[i & 1][3], mva); }
    };
    auto modw = [&](auto I, auto T) {
      constexpr int i = decltype(I)::v, t = decltype(T)::v, q = i & 1;
      const u32x4 x = xr[i][t];
      u32x4 o;
#pragma unroll
      for (int e = 0; e < 4; ++e)
        o[e] = mod_pair(x[e], mv[q][e >> 1][(e & 1) * 2], mv[q][2 + (e >> 1)][(e & 1) * 2], mv[q][e >> 1][(e & 1) * 2 + 1], mv[q][2 + (e >> 1)][(e & 1) * 2 + 1]);
      *(u32x4*)(hw + ((2 * i) % K1_NH) * 8192 + t * 2048) = o;
    };
    rdm(IC<0>{});
    sfor<0, 12>([&](auto I) {
      constexpr int i = decltype(I)::v;
      constexpr int ahead = imin(HD - 1, 11 - i);
      WAIT_VM(2 * ahead + 1); WAIT_LG(i ? 1 : 0);
      modw(I, IC<0>{});
      ldx(IC<i + HD>{}, IC<0>{});
      rdm(IC<i + 1>{});
      if constexpr (i > 0) { WAIT_LG(i < 11 ? 5 : 1); if (i > 1 || j >= 2) BAR(); }
      WAIT_VM(2 * ahead + (i + HD < 12 ? 1 : 0));
      modw(I, IC<1>{});
      ldx(IC<i + HD>{}, IC<1>{});
      WAIT_LG(i ? 1 : 0);
      BAR();
    });
    WAIT_LG(0);
    BAR();
    if (j < 2) BAR();
    return;
  }
  ldm(); WAIT_VM(0); stm();
  STAMP(2);
  f32x16 acc[5];
  {
    const int blk = (lane >> 4) & 1, q4 = (lane & 15) >> 2, p = lane & 3;
    unsigned rw = lds0 + (8 * h + q4) * 320 + blk * 32 + p * 8, rw1 = rw + 5 * SLOT_B;
    unsigned rh = lds0 + K1_H + wave * 1024 + l16;
    tie(rw, rw1, rh);
    u32x4 wf[2][5], hh[2];
    auto rdw = [&](auto S, auto C) {
      constexpr int s = decltype(S)::v, c = decltype(C)::v, cur = s & 1, slot = (s >> 1) % K1_NW, so = (slot % 5) * SLOT_B + (s & 1) * 5120;
      if constexpr (s < KS) {
        if constexpr (c == 0) lds_128<((s >> 1) % K1_NH) * 8192 + (s & 1) * 4096>(hh[cur], rh);
        lds_tr2<so + c * 64, so + c * 64 + 1280>(wf[cur][c], slot < 5 ? rw : rw1);
      }
    };
    STAMP(3);
    BAR();
    STAMP(4);
    sfor<0, 5>([&](auto C) { rdw(IC<0>{}, C); });
    sfor<0, KS>([&](auto S) {
      constexpr int s = decltype(S)::v, cur = s & 1;
      constexpr bool more = s + 1 < KS;
      if constexpr ((s & 1) && more) BAR();
      sfor<0, 5>([&](auto C) {
        constexpr int c = decltype(C)::v;
        WAIT_LG((4 - c) * 2 + (more ? 2 * c + (c > 0) : 0));
        mfma_v<s == 0>(acc[c], wf[cur][c], hh[cur]);
        rdw(IC<s + 1>{}, C);
      });
    });
    asm volatile("s_nop 15" : "+v"(acc[0]), "+v"(acc[1]), "+v"(acc[2]), "+v"(acc[3]), "+v"(acc[4]));
  }
  STAMP(16);
  const int tt = sel(txt, (wave & 1) * TPB + 32, rowhalf * TPB + (idx & 7) * 4 + wave);
  if (txt && wave >= 2) return;
#pragma unroll
  for (int c = 0; c < 5; ++c) {
    const int f0 = n0 + c * 32;
    if (ct == 14 && c < 3) continue;
    const int mat = f0 / D, st = (f0 % D) >> 4;
    char* dst = ws + WS_QKV + ((size_t)mat * NTILE + tt) * TILE_B + st * 1024 + lane * 16;
    gst16(dst, pack8(acc[c], 0));
    gst16(dst + 1024, pack8(acc[c], 1));
  }
  STAMP(17); WAIT_VM(0); STAMP(18);
  STAMP_OUT(1); RT_OUT(1);
}

__global__ __launch_bounds__(384, 2) void k2_sv(const char* __restrict__ wo_img, char* __restrict__ ws, const unsigned short* __restrict__ cvec,
                                                const unsigned short* __restrict__ wmod_img, const unsigned short* __restrict__ wmod_txt) {
  extern __shared__ __attribute__((aligned(16))) char smem[];
  STAMP_DECL; STAMP(0); RT_IN(2);
  const int bid = blockIdx.x, xcd = bid & 7, idx = bid >> 3;
  const int b = xcd >> 2, sub = xcd & 3, qh = sub & 1, ch = sub >> 1;
  int wave = __builtin_amdgcn_readfirstlane(threadIdx.x >> 6);
  const int lane = threadIdx.x & 63;
  const int nq = 5 - qh, nS = nq * (4 - ch);
  const unsigned lds0 = (unsigned)(size_t)(LAS char*)smem;
  const unsigned l16 = lane * 16;
  const bool isS = idx < nS;
  const int tv = (int)((0x1e120700u >> (sub * 8)) & 0xff) + idx - nS;
  const int dv = sel(isS, idx * (13 + 3 * qh), tv * 13) >> 6;
  const int pb = sel(isS, qh * 5 + idx - dv * nq, dv);
  const int sc = sel(isS, ch * 4 + dv, tv - dv * 5);
  const int kt0 = sel(sc < 6, sc * 5, 28);
  const int n0 = sel(sc < 4, sc * 160, 608);
  const int tl = sel(pb < 8, pb * 4 + (wave & 3), 32);
  const char* priv = ws + WS_QKV + (unsigned)((sel(isS, 0, 2 * NTILE) + b * TPB + tl) * TILE_B);
  const char* base = sel(isS, (const char*)ws + WS_QKV + (unsigned)((NTILE + b * TPB + kt0) * TILE_B + (wave & 1) * 1024), wo_img + (n0 * 2 + (wave & 1) * 16 * 1536));
  u32x4 xr[KS];
  asm volatile("s_nop 4" ::"s"(priv), "s"(base) : "memory");
  if (idx >= 27) {
    if (idx < 30) {
      const int g = (idx - 27) * 8 + xcd, type = g & 1, slab = 24 + (g >> 1);
      mod_slab<384, D>(type ? wmod_txt : wmod_img, cvec, 0, slab * 64, (float*)(ws + WS_MOD) + (size_t)type * MSPLIT * 2 * 2304 + slab * 64, (float*)smem);
    }
    return;
  }
  if (wave < 4) priv_prologue(xr, priv, l16);
  asm volatile("; AUDIT_SAVE" : "+s"(wave));
  if (__builtin_expect(wave >= 4, 1)) {
    unsigned voff[5];
    w_voff(voff, lane, 1536);
    if (isS) {
#pragma unroll
      for (int i = 0; i < 5; ++i) voff[i] = i * TILE_B + l16 - (i < 4 ? i * 1024 : 0);
    }
    loader_shared<NSLOT, false>(base, voff, isS ? 2048 : 32 * 1536, lds0 + (wave - 4) * 5120);
    return;
  }
  STAMP(1);
  f32x16 acc[5];
  if (isS) {
    asm volatile("; AUDIT_RESTORE");
    consumer<2>(acc, xr, priv, lds0, lane, 0 STAMP_PASS);
    STAMP(16);
    if (pb == 8 && wave) return;
    const int own0 = sc < 6 ? 0 : 2, qt = b * TPB + tl;
    float m = -3.0e38f;
#pragma unroll
    for (int c = 0; c < 5; ++c)
      if (c >= own0) {
#pragma unroll
        for (int r = 0; r < 16; ++r) m = fmaxf(m, acc[c][r]);
      }
    m = xhalf_max(m);
    const float mc = -m * SM_C;
    float l = 0.f;
    char* dst = ws + WS_P + (size_t)qt * PT_B + (size_t)kt0 * 2048 + lane * 16;
#pragma unroll
    for (int c = 0; c < 5; ++c)
      if (c >= own0) {
#pragma unroll
        for (int r = 0; r < 16; ++r) { const float pv = __builtin_amdgcn_exp2f(fmaf(acc[c][r], SM_C, mc)); acc[c][r] = pv; l += pv; }
        gst16(dst + c * 2048, pack8(acc[c], 0));
        gst16(dst + c * 2048 + 1024, pack8(acc[c], 1));
      }
    l = xhalf_sum(l);
    float* st = (float*)(ws + WS_ST) + ((size_t)qt * NCH + sc) * 64;
    st[lane] = lane < 32 ? m : l;
    STAMP(17); WAIT_VM(0); STAMP(18);
    STAMP_OUT(2); RT_OUT(2);
  } else {
    asm volatile("; AUDIT_RESTORE");
    consumer<1>(acc, xr, priv, lds0, lane, 0 STAMP_PASS);
    STAMP(16);
    if (pb == 8 && wave) return;
#pragma unroll
    for (int c = 0; c < 5; ++c) {
      if (sc == 4 && c < 1) continue;
      const int dt = (n0 >> 5) + c;
      char* dst = ws + WS_VP + ((size_t)b * 24 + dt) * PT_B + (size_t)tl * 2048 + lane * 16;
      gst16(dst, pack8(acc[c], 0));
      gst16(dst + 1024, pack8(acc[c], 1));
    }
    RT_OUT(2);
  }
}

constexpr int NSTG3 = 22;
constexpr int LDS_G3 = 2 * PT_B;
constexpr int LDS_K3 = LDS_G3 + 256;
constexpr int ld3_cnt(int k) { return imin(NSTG3, k + 5); }
DI void chunk_scales(float (&f)[NCH], const float (&mm)[NCH], const float (&ll)[NCH]) {
  float m = mm[0];
#pragma unroll
  for (int c = 1; c < NCH; ++c) m = fmaxf(m, mm[c]);
  float l = 0.f;
#pragma unroll
  for (int c = 0; c < NCH; ++c) { f[c] = __builtin_amdgcn_exp2f((mm[c] - m) * SM_C); l = fmaf(f[c], ll[c], l); }
  const float inv = 1.f / l;
#pragma unroll
  for (int c = 0; c < NCH; ++c) f[c] *= inv;
}

__global__ __launch_bounds__(384, 2) void k3_out(const char* __restrict__ x_img, const char* __restrict__ x_txt, const char* __restrict__ wo_txt,
                                                 char* __restrict__ ws, char* __restrict__ out) {
  extern __shared__ __attribute__((aligned(16))) char smem[];
  STAMP_DECL; STAMP(0); RT_IN(3);
  const int bid = blockIdx.x, xcd = bid & 7, idx = bid >> 3;
  const int b = xcd >> 2, sub = xcd & 3;
  const int wave = __builtin_amdgcn_readfirstlane(threadIdx.x >> 6), lane = threadIdx.x & 63;
  const int r = lane & 31, h = lane >> 5;
  const unsigned lds0 = (unsigned)(size_t)(LAS char*)smem;
  const unsigned l16 = lane * 16;
  if (idx >= 30) return;
  if (idx >= 6) {
    const int i = idx - 6, qb = (sub & 1) * 4 + (i & 3), dp = (sub >> 1) * 6 + (i >> 2);
    if (wave >= 4) {
      const char* p = ws + WS_VP + ((size_t)b * 24 + dp * 2 + (wave - 4)) * PT_B;
      const unsigned lds = lds0 + (wave - 4) * PT_B;
      asm volatile("s_nop 4" ::: "memory");
      sfor<0, NSTG3>([&](auto K) {
        constexpr int k = decltype(K)::v;
        sfor<(k == 0 ? 0 : ld3_cnt(k - 1)), ld3_cnt(k)>([&](auto G) { constexpr int g = decltype(G)::v; gldsN<3>(l16, p + g * 3072, lds + g * 3072); });
        WAIT_VM(3 * (ld3_cnt(k) - k - 1)); BAR();
      });
      return;
    }
    const int qt = b * TPB + qb * 4 + wave;
    float mm[NCH], ll[NCH], f[NCH], g0;
    asm volatile("s_nop 4" ::: "memory");
    {
      const char* stp = ws + WS_ST + (size_t)qt * NCH * 256;
      sfor<0, NCH>([&](auto C) { constexpr int c = decltype(C)::v; gld4<c * 256>(mm[c], r * 4, stp); gld4<c * 256 + 128>(ll[c], r * 4, stp); });
      const char* gp = ws + WS_MOD + ((size_t)b * 2304 + 1536 + dp * 64) * 4;
      gld4<0>(g0, lane * 4, gp);
    }
    const char* pb = ws + WS_P + (size_t)qt * PT_B;
    u32x4 pf[KSK];
    auto ldp = [&](auto S) { constexpr int s = decltype(S)::v; if constexpr (s < KSK) gld16<(s & 3) * 1024>(pf[s], l16, pb + (s >> 2) * 4096); };
    sfor<0, RAHEAD>([&](auto S) { ldp(S); });
    WAIT_VM(RAHEAD);
    tie(mm[0], mm[1], mm[2], mm[3], mm[4], mm[5], mm[6], ll[0], ll[1], ll[2], ll[3], ll[4], ll[5], ll[6], g0);
    if (wave == 0) *(float*)(smem + LDS_G3 + lane * 4) = g0;
    chunk_scales(f, mm, ll);
    f32x16 acc[2], tmp[2];
    acc[0] = f32x16{}; acc[1] = f32x16{};
    tie(acc[0], acc[1], f[0], f[1], f[2], f[3], f[4], f[5], f[6]);
    u32x4 vf[2][2];
    const unsigned rv0 = lds0 + l16, rv1 = rv0 + PT_B;
    auto rdv = [&](auto S, auto T) {
      constexpr int s = decltype(S)::v, t = decltype(T)::v;
      if constexpr (s < 64) lds_128<s * 1024>(vf[s & 1][t], t ? rv1 : rv0);
      else if constexpr (s < KSK) lds_128<(s - 2) * 1024>(vf[s & 1][t], (t ? rv1 : rv0) + 2048);
    };
    u32x4 xv[4];
    const char* xb = x_img + (((size_t)b * NI + (qb * 4 + wave) * 32) * D + dp * 64) * 2;
    const unsigned xo = (lane >> 3) * (D * 2) + (lane & 7) * 16;
    WAIT_LG0();
    STAMP(3);
    BAR();
    STAMP(4);
    rdv(IC<0>{}, IC<0>{}); rdv(IC<0>{}, IC<1>{}); rdv(IC<1>{}, IC<0>{}); rdv(IC<1>{}, IC<1>{});
    sfor<0, KSK>([&](auto S) {
      constexpr int s = decltype(S)::v, cur = s & 1, c = s / 10, cs = s % 10;
      if constexpr ((s + 2) % 3 == 0 && s + 2 < KSK) BAR();
      WAIT_VM(imin(RAHEAD - 1, KSK - 1 - s) + (s > KSK - 1 - RAHEAD ? 4 : 0)); tie(pf[s]);
      sfor<0, 2>([&](auto T) {
        constexpr int t = decltype(T)::v;
        WAIT_LG((1 - t) + (s + 1 < KSK ? 2 : 0) + (s + 2 < KSK ? t : 0)); tie(vf[cur][t]);
        if constexpr (cs == 0) tmp[t] = mfma(vf[cur][t], pf[s], f32x16{});
        else tmp[t] = mfma(vf[cur][t], pf[s], tmp[t]);
        rdv(IC<s + 2>{}, T);
        if constexpr (t == 0) {
          ldp(IC<s + RAHEAD>{});
          if constexpr (s == KSK - 1 - RAHEAD) { gld16<0>(xv[0], xo, xb); gld16<0>(xv[1], xo, xb + 8 * D * 2); gld16<0>(xv[2], xo, xb + 16 * D * 2); gld16<0>(xv[3], xo, xb + 24 * D * 2); }
        }
        SBAR();
      });
      if constexpr (cs == 9 || s == KSK - 1) {
#pragma unroll
        for (int e = 0; e < 16; ++e) { acc[0][e] = fmaf(tmp[0][e], f[c], acc[0][e]); acc[1][e] = fmaf(tmp[1][e], f[c], acc[1][e]); }
        tie(acc[0], acc[1]);
        SBAR();
      }
    });
    WAIT_VM(0); tie(xv[0], xv[1], xv[2], xv[3]);
    BAR();
    STAMP(16);
    const size_t orow = ((size_t)b * NI + (qb * 4 + wave) * 32 + (lane >> 3)) * (D * 2) + dp * 128 + (lane & 7) * 16;
    {
      char* tb = smem + wave * 8192;
#pragma unroll
      for (int e = 0; e < 8; ++e) {
        const int t = e >> 2, gq = e & 3;
        const f32x4 gt = *(const f32x4*)(smem + LDS_G3 + (e * 8 + 4 * h) * 4);
        const f32x4 y = {gt[0] * acc[t][4 * gq], gt[1] * acc[t][4 * gq + 1], gt[2] * acc[t][4 * gq + 2], gt[3] * acc[t][4 * gq + 3]};
        *(f32x4*)(tb + r * 256 + (((2 * e + h) ^ (r & 15)) * 16)) = y;
      }
#pragma unroll
      for (int e = 0; e < 4; ++e) {
        const int row = e * 8 + (lane >> 3), c2 = (lane & 7) * 2;
        const f32x4 y0 = *(const f32x4*)(tb + row * 256 + ((c2 ^ (row & 15)) * 16));
        const f32x4 y1 = *(const f32x4*)(tb + row * 256 + (((c2 + 1) ^ (row & 15)) * 16));
        u32x4 o = {pk_bf16(bf_lo(xv[e][0]) + y0[0], bf_hi(xv[e][0]) + y0[1]), pk_bf16(bf_lo(xv[e][1]) + y0[2], bf_hi(xv[e][1]) + y0[3]),
                   pk_bf16(bf_lo(xv[e][2]) + y1[0], bf_hi(xv[e][2]) + y1[1]), pk_bf16(bf_lo(xv[e][3]) + y1[2], bf_hi(xv[e][3]) + y1[3])};
        *(u32x4*)(out + orow + (size_t)e * 8 * D * 2) = o;
      }
    }
    STAMP(17); WAIT_VM(0); STAMP(18);
    STAMP_OUT(3); RT_OUT(3);
  } else {
    if (wave >= 4) return;
    asm volatile("s_nop 4" ::: "memory");
    const int T = sub * 6 + idx;
    const int qt = b * TPB + 32;
    constexpr int LV = 0, LW = 33 * 2048, LR = LW + 768 * 64;
    float mm[NCH], ll[NCH];
    const char* stp = uni(ws + WS_ST + (size_t)qt * NCH * 256);
    sfor<0, NCH>([&](auto C) { constexpr int c = decltype(C)::v; gld4<c * 256>(mm[c], r * 4, stp); gld4<c * 256 + 128>(ll[c], r * 4, stp); });
    const char* vsrc = uni(ws + WS_QKV + ((size_t)2 * NTILE + b * TPB) * TILE_B + T * 2048);
    for (int kt = wave; kt < 33; kt += 4) gldsN<2>(l16, vsrc + (size_t)kt * TILE_B, lds0 + LV + kt * 2048);
    const int ca = wave, cb = wave < 3 ? wave + 4 : 3;
    u32x4 pa[10], pc[10];
    const char* pA = uni(ws + WS_P + (size_t)qt * PT_B + ca * 10240);
    const char* pC = uni(ws + WS_P + (size_t)qt * PT_B + (cb == 6 ? 56 * 1024 : cb * 10240));
    sfor<0, 10>([&](auto S) { constexpr int s = decltype(S)::v; gld16<(s & 3) * 1024>(pa[s], l16, pA + (s >> 2) * 4096); });
    sfor<0, 10>([&](auto S) { constexpr int s = decltype(S)::v; gld16<(s & 3) * 1024>(pc[s], l16, pC + (s >> 2) * 4096); });
    WAIT_VM(0);
    tie(mm[0], mm[1], mm[2], mm[3], mm[4], mm[5], mm[6], ll[0], ll[1], ll[2], ll[3], ll[4], ll[5], ll[6]);
    tie(pa[0], pa[1], pa[2], pa[3], pa[4], pa[5], pa[6], pa[7], pa[8], pa[9]);
    tie(pc[0], pc[1], pc[2], pc[3], pc[4], pc[5], pc[6], pc[7], pc[8], pc[9]);
    {
      const char* wsrc = uni(wo_txt + (size_t)(192 * wave) * 1536 + T * 64);
      const unsigned wv = (lane >> 2) * 1536 + (lane & 3) * 16;
      for (int i = 0; i < 12; ++i) gldsN<1>(wv, wsrc + (size_t)i * 16 * 1536, lds0 + LW + (192 * wave + 16 * i) * 64);
    }
    __builtin_amdgcn_s_barrier();
    float f[NCH];
    chunk_scales(f, mm, ll);
    float fa = f[0], fc = f[4];
    if (wave == 1) { fa = f[1]; fc = f[5]; }
    if (wave == 2) { fa = f[2]; fc = f[6]; }
    if (wave == 3) { fa = f[3]; fc = 0.f; }
    const int blk = (lane >> 4) & 1, q4 = (lane & 15) >> 2, p = lane & 3;
    const unsigned va = lds0 + LV + blk * 1024 + ((p & 1) * 32 + 4 * h + q4) * 16 + (p >> 1) * 8;
    f32x16 ta = f32x16{}, tc = f32x16{};
    {
      const unsigned a0 = va + ca * 10240, a1 = va + (cb == 6 ? 56 * 1024 : cb * 10240);
      const int skip = cb == 6 ? 4 : 0;
      sfor<0, 10>([&](auto S) {
        constexpr int s = decltype(S)::v, o = (s >> 1) * 2048 + (s & 1) * 256;
        u32x2 lo0, hi0, lo1, hi1;
        lds_tr<o>(lo0, a0); lds_tr<o + 128>(hi0, a0); lds_tr<o>(lo1, a1); lds_tr<o + 128>(hi1, a1);
        WAIT_LG0(); tie(lo0, hi0, lo1, hi1);
        ta = mfma(u32x4{lo0[0], lo0[1], hi0[0], hi0[1]}, pa[s], ta);
        if (s >= skip) tc = mfma(u32x4{lo1[0], lo1[1], hi1[0], hi1[1]}, pc[s], tc);
      });
    }
#pragma unroll
    for (int e = 0; e < 16; ++e) ta[e] = ta[e] * fa + (wave == 3 ? 0.f : tc[e] * fc);
    f32x4* red = (f32x4*)(smem + LR);
    if (wave > 0) {
#pragma unroll
      for (int e = 0; e < 4; ++e) red[((wave - 1) * 4 + e) * 64 + lane] = f32x4{ta[4 * e], ta[4 * e + 1], ta[4 * e + 2], ta[4 * e + 3]};
    }
    WAIT_LG0();
    __builtin_amdgcn_s_barrier();
    GAS unsigned* cnt = (GAS unsigned*)(ws + WS_FLAG + b * 256);
    if (wave == 0) {
#pragma unroll
      for (int w = 0; w < 3; ++w)
#pragma unroll
        for (int e = 0; e < 4; ++e) { const f32x4 v = red[(w * 4 + e) * 64 + lane]; ta[4 * e] += v[0]; ta[4 * e + 1] += v[1]; ta[4 * e + 2] += v[2]; ta[4 * e + 3] += v[3]; }
      const u32x4 o0 = pack8(ta, 0), o1 = pack8(ta, 1);
      char* ab = ws + WS_AT + (size_t)b * TILE_B + T * 2048 + l16;
      asm volatile("global_store_dwordx4 %0, %1, off sc1\n\tglobal_store_dwordx4 %0, %2, off offset:1024 sc1\n\ts_nop 1\n\ts_waitcnt vmcnt(0)"
                   ::"v"(ab), "v"(o0), "v"(o1) : "memory");
      if (lane == 0) __hip_atomic_fetch_add(cnt, 1u, __ATOMIC_RELAXED, __HIP_MEMORY_SCOPE_AGENT);
      for (unsigned spins = 0; spins < (1u << 22); ++spins) {
        if ((unsigned)__builtin_amdgcn_readfirstlane(__hip_atomic_load(cnt, __ATOMIC_RELAXED, __HIP_MEMORY_SCOPE_AGENT)) >= 24u) break;
        __builtin_amdgcn_s_sleep(1);
      }
    }
    asm volatile("s_waitcnt vmcnt(0) lgkmcnt(0)" ::: "memory");
    __builtin_amdgcn_s_barrier();
    u32x4 af[12];
    const char* ab = uni(ws + WS_AT + (size_t)b * TILE_B + wave * 12288);
    sfor<0, 12>([&](auto S) { constexpr int s = decltype(S)::v; gld16_sc1<(s & 3) * 1024>(af[s], l16, ab + (s >> 2) * 4096); });
    u32x2 xv[4]; u32x4 g0[4];
    {
      const char* xb = uni(x_txt + ((size_t)b * NTX * D + T * 32) * 2);
      const unsigned xo = (r * D + 4 * h) * 2;
      sfor<0, 4>([&](auto E) { constexpr int e = decltype(E)::v; gld8<e * 16>(xv[e], xo, xb); });
      const char* gb = uni(ws + WS_MOD + ((size_t)(MSPLIT * 2 + b) * 2304 + 1536 + T * 32) * 4);
      sfor<0, 4>([&](auto E) { constexpr int e = decltype(E)::v; gld16<e * 32>(g0[e], h * 16, gb); });
    }
    WAIT_VM(0);
    tie(af[0], af[1], af[2], af[3], af[4], af[5], af[6], af[7], af[8], af[9], af[10], af[11]);
    tie(xv[0], xv[1], xv[2], xv[3], g0[0], g0[1], g0[2], g0[3]);
    f32x16 tb = f32x16{};
    {
      const unsigned wa = lds0 + LW + (192 * wave + 4 * h + q4) * 64 + blk * 32 + p * 8;
      sfor<0, 12>([&](auto S) {
        constexpr int s = decltype(S)::v;
        u32x2 lo, hi;
        lds_tr<s * 1024>(lo, wa); lds_tr<s * 1024 + 512>(hi, wa);
        WAIT_LG0(); tie(lo, hi);
        tb = mfma(u32x4{lo[0], lo[1], hi[0], hi[1]}, af[s], tb);
      });
    }
    if (wave > 0) {
#pragma unroll
      for (int e = 0; e < 4; ++e) red[((wave - 1) * 4 + e) * 64 + lane] = f32x4{tb[4 * e], tb[4 * e + 1], tb[4 * e + 2], tb[4 * e + 3]};
    }
    WAIT_LG0();
    __builtin_amdgcn_s_barrier();
    if (wave == 0) {
      char* ob = out + ((size_t)2 * NI * D + ((size_t)b * NTX + r) * D + T * 32 + 4 * h) * 2;
#pragma unroll
      for (int e = 0; e < 4; ++e) {
        f32x4 o = {tb[4 * e], tb[4 * e + 1], tb[4 * e + 2], tb[4 * e + 3]};
#pragma unroll
        for (int w = 0; w < 3; ++w) o += red[(w * 4 + e) * 64 + lane];
        const f32x4 gt = __builtin_bit_cast(f32x4, g0[e]);
        u32x2 ov = {pk_bf16(fmaf(gt[0], o[0], bf_lo(xv[e][0])), fmaf(gt[1], o[1], bf_hi(xv[e][0]))),
                    pk_bf16(fmaf(gt[2], o[2], bf_lo(xv[e][1])), fmaf(gt[3], o[3], bf_hi(xv[e][1])))};
        *(u32x2*)(ob + e * 16) = ov;
      }
      RT_OUT(3);
    }
  }
}

extern "C" void kernel_launch(void* const* d_in, const int* in_sizes, int n_in, void* d_out, int out_size, void* d_ws, size_t ws_size,
                              hipStream_t stream) {
  (void)in_sizes; (void)n_in; (void)out_size; (void)ws_size;
  const unsigned short* x_img = (const unsigned short*)d_in[0];
  const unsigned short* x_txt = (const unsigned short*)d_in[1];
  const unsigned short* cvec = (const unsigned short*)d_in[2];
  char* ws = (char*)d_ws;
  (void)hipFuncSetAttribute((const void*)k1_qkv, hipFuncAttributeMaxDynamicSharedMemorySize, LDS_K1);
  (void)hipFuncSetAttribute((const void*)k2_sv, hipFuncAttributeMaxDynamicSharedMemorySize, LDS_GEMM);
  (void)hipFuncSetAttribute((const void*)k3_out, hipFuncAttributeMaxDynamicSharedMemorySize, LDS_K3);
  k0_prep<<<K0_GRID, 256, 0, stream>>>(x_img, x_txt, cvec, (const unsigned short*)d_in[7], (const unsigned short*)d_in[8],
                                       (const char*)d_in[3], (const char*)d_in[4], ws);
  k1_qkv<<<256, 960, LDS_K1, stream>>>((const char*)d_in[3], (const char*)d_in[4], (const char*)d_in[5], (const char*)d_in[6], ws);
  k2_sv<<<256, 384, LDS_GEMM, stream>>>((const char*)d_in[5], ws, cvec, (const unsigned short*)d_in[7], (const unsigned short*)d_in[8]);
  k3_out<<<256, 384, LDS_K3, stream>>>((const char*)d_in[0], (const char*)d_in[1], (const char*)d_in[6], ws, (char*)d_out);
}
```

```cpp
#include <hip/hip_runtime.h>
#include <hip/hip_bf16.h>
#include <stdint.h>

typedef short    bf16x8 __attribute__((ext_vector_type(8)));
typedef float    f32x16 __attribute__((ext_vector_type(16)));
typedef float    f32x4  __attribute__((ext_vector_type(4)));
typedef float    f32x2  __attribute__((ext_vector_type(2)));
typedef unsigned u32x4  __attribute__((ext_vector_type(4)));
typedef unsigned u32x2  __attribute__((ext_vector_type(2)));
typedef __bf16   bf16x2_t __attribute__((ext_vector_type(2)));

#define DI __device__ __forceinline__
#define LAS __attribute__((address_space(3)))
#define GAS __attribute__((address_space(1)))
#define SBAR() __builtin_amdgcn_sched_barrier(0)

constexpr int D = 768, NI = 1024, NTX = 32;
constexpr int TPB = 33;
constexpr int NTILE = 66;
constexpr int KS = 48;
constexpr int TILE_B = KS * 1024;
constexpr int KSK = 66;
constexpr int PT_B = KSK * 1024;
constexpr int NCH = 7;
constexpr int MSPLIT = 4;

constexpr size_t WS_FLAG = 0;
constexpr int F_QKV = 0, F_P = 51, F_VP = 69, F_GATE = 79, F_TXT = 103, F_N = 128;
constexpr size_t WS_MOD  = 32768;
constexpr size_t WS_XN   = WS_MOD + (size_t)2 * MSPLIT * 2 * 2304 * 4;
constexpr size_t WS_QKV  = WS_XN + (size_t)NTILE * TILE_B;
constexpr size_t WS_P    = WS_QKV + (size_t)3 * NTILE * TILE_B;
constexpr size_t WS_VP   = WS_P + (size_t)NTILE * PT_B;
constexpr size_t WS_ST   = WS_VP + (size_t)48 * PT_B;
constexpr size_t WS_AT   = WS_ST + (size_t)NTILE * NCH * 2 * 32 * 4;
constexpr size_t WS_END  = WS_AT + (size_t)2 * TILE_B;

constexpr float SM_C = 0.03608439182435161f * 1.4426950408889634f;

template <int I> struct IC { static constexpr int v = I; };
template <int B, int E, class F> DI void sfor(F&& f) { if constexpr (B < E) { f(IC<B>{}); sfor<B + 1, E>(f); } }

DI unsigned pk_bf16(float a, float b) { f32x2 v = {a, b}; bf16x2_t r = __builtin_convertvector(v, bf16x2_t); return __builtin_bit_cast(unsigned, r); }
DI float bf_lo(unsigned u) { return __uint_as_float(u << 16); }
DI float bf_hi(unsigned u) { return __uint_as_float(u & 0xffff0000u); }
DI f32x16 mfma(u32x4 a, u32x4 b, f32x16 c) {
  return __builtin_amdgcn_mfma_f32_32x32x16_bf16(__builtin_bit_cast(bf16x8, a), __builtin_bit_cast(bf16x8, b), c, 0, 0, 0);
}
template <bool FIRST> DI void mfma_v(f32x16& c, u32x4 a, u32x4 b) {
  if constexpr (FIRST) asm volatile("v_mfma_f32_32x32x16_bf16 %0, %1, %2, 0" : "=&v"(c) : "v"(a), "v"(b));
  else asm volatile("v_mfma_f32_32x32x16_bf16 %0, %1, %2, %0" : "+v"(c) : "v"(a), "v"(b));
}
DI float xhalf_max(float v) { auto r = __builtin_amdgcn_permlane32_swap(__float_as_uint(v), __float_as_uint(v), false, false); return fmaxf(__uint_as_float(r[0]), __uint_as_float(r[1])); }
DI float xhalf_sum(float v) { auto r = __builtin_amdgcn_permlane32_swap(__float_as_uint(v), __float_as_uint(v), false, false); return __uint_as_float(r[0]) + __uint_as_float(r[1]); }
DI u32x4 pack8(const f32x16& a, int s) {
  u32x4 r = {pk_bf16(a[8 * s], a[8 * s + 1]), pk_bf16(a[8 * s + 2], a[8 * s + 3]), pk_bf16(a[8 * s + 4], a[8 * s + 5]), pk_bf16(a[8 * s + 6], a[8 * s + 7])};
  return r;
}
DI unsigned mod_pair(unsigned x, float s0, float t0, float s1, float t1) {
  unsigned o, a, b;
  asm volatile("v_lshlrev_b32 %1, 16, %3\n\tv_and_b32 %2, 0xffff0000, %3\n\tv_fma_f32 %1, %1, %4, %5\n\tv_fma_f32 %2, %2, %6, %7\n\tv_cvt_pk_bf16_f32 %0, %1, %2"
      : "=v"(o), "=&v"(a), "=&v"(b) : "v"(x), "v"(s0), "v"(t0), "v"(s1), "v"(t1));
  return o;
}
DI const char* sadd(const char* p, unsigned d) {
  const unsigned long long u = (unsigned long long)p;
  unsigned lo, hi;
  asm("s_add_u32 %0, %2, %4\n\ts_addc_u32 %1, %3, 0" : "=&s"(lo), "=&s"(hi) : "s"((unsigned)u), "s"((unsigned)(u >> 32)), "s"(d) : "scc");
  return (const char*)(((unsigned long long)hi << 32) | lo);
}
DI const char* uni(const char* p) {
  unsigned long long u = (unsigned long long)p;
  unsigned lo = __builtin_amdgcn_readfirstlane((unsigned)u), hi = __builtin_amdgcn_readfirstlane((unsigned)(u >> 32));
  return (const char*)(((unsigned long long)hi << 32) | lo);
}

#define STAMP(i)
#define STAMP_DECL
#define STAMP_OUT(k)
#define STAMP_ARG
#define STAMP_PASS
#define RT_IN(k)
#define RT_OUT(k)
#define RT_MID(i)

#define ST_POL "sc1"
DI void gst16(char* p, u32x4 v) { asm volatile("global_store_dwordx4 %0, %1, off " ST_POL "\n\ts_nop 1" ::"v"(p), "v"(v) : "memory"); }
#define WAIT_VM(n) asm volatile("s_waitcnt vmcnt(%0)" ::"n"(n) : "memory")
#define WAIT_LG0() asm volatile("s_waitcnt lgkmcnt(0)" ::: "memory")
template <int OFF> DI void gld16(u32x4& d, unsigned voff, const char* sb) {
  asm volatile("global_load_dwordx4 %0, %1, %2 offset:%3" : "=v"(d) : "v"(voff), "s"(sb), "i"(OFF) : "memory");
}
template <int OFF> DI void gld16a(u32x4& d, unsigned voff, const char* sb) {
  asm volatile("global_load_dwordx4 %0, %1, %2 offset:%3" : "=a"(d) : "v"(voff), "s"(sb), "i"(OFF) : "memory");
}
template <int OFF> DI void gld8(u32x2& d, unsigned voff, const char* sb) {
  asm volatile("global_load_dwordx2 %0, %1, %2 offset:%3" : "=v"(d) : "v"(voff), "s"(sb), "i"(OFF) : "memory");
}
template <int OFF> DI void gld4(float& d, unsigned voff, const char* sb) {
  asm volatile("global_load_dword %0, %1, %2 offset:%3 sc1" : "=v"(d) : "v"(voff), "s"(sb), "i"(OFF) : "memory");
}
template <int OFF> DI void touch16(u32x4& junk, unsigned voff, const char* sb) {
  asm volatile("global_load_dwordx4 %0, %1, %2 offset:%3" : "+v"(junk) : "v"(voff), "s"(sb), "i"(OFF) : "memory");
}
DI void touch(unsigned& junk, unsigned voff, const char* sb) {
  asm volatile("global_load_dword %0, %1, %2" : "+v"(junk) : "v"(voff), "s"(sb) : "memory");
}
template <int OFF> DI void gld16_sc1(u32x4& d, unsigned voff, const char* sb) {
  asm volatile("global_load_dwordx4 %0, %1, %2 offset:%3 sc1" : "=v"(d) : "v"(voff), "s"(sb), "i"(OFF) : "memory");
}
DI void glds5(const unsigned (&v)[5], const char* sb, unsigned lds) {
  asm volatile(
      "s_mov_b32 m0, %6\n\ts_nop 0\n\t"
      "global_load_lds_dwordx4 %0, %5 sc1\n\t"
      "global_load_lds_dwordx4 %1, %5 offset:1024 sc1\n\t"
      "global_load_lds_dwordx4 %2, %5 offset:2048 sc1\n\t"
      "global_load_lds_dwordx4 %3, %5 offset:3072 sc1\n\t"
      "s_add_u32 m0, m0, 4096\n\ts_nop 0\n\t"
      "global_load_lds_dwordx4 %4, %5 sc1"
      ::"v"(v[0]), "v"(v[1]), "v"(v[2]), "v"(v[3]), "v"(v[4]), "s"(sb), "s"(lds) : "memory", "scc");
}
template <int N> DI void gldsN(unsigned v, const char* sb, unsigned lds) {
  static_assert(N >= 1 && N <= 4, "");
  if constexpr (N == 1) asm volatile("s_mov_b32 m0, %2\n\ts_nop 0\n\tglobal_load_lds_dwordx4 %0, %1 sc1" ::"v"(v), "s"(sb), "s"(lds) : "memory");
  if constexpr (N == 2) asm volatile("s_mov_b32 m0, %2\n\ts_nop 0\n\tglobal_load_lds_dwordx4 %0, %1 sc1\n\tglobal_load_lds_dwordx4 %0, %1 offset:1024 sc1" ::"v"(v), "s"(sb), "s"(lds) : "memory");
  if constexpr (N == 3) asm volatile("s_mov_b32 m0, %2\n\ts_nop 0\n\tglobal_load_lds_dwordx4 %0, %1 sc1\n\tglobal_load_lds_dwordx4 %0, %1 offset:1024 sc1\n\tglobal_load_lds_dwordx4 %0, %1 offset:2048 sc1" ::"v"(v), "s"(sb), "s"(lds) : "memory");
  if constexpr (N == 4) asm volatile("s_mov_b32 m0, %2\n\ts_nop 0\n\tglobal_load_lds_dwordx4 %0, %1 sc1\n\tglobal_load_lds_dwordx4 %0, %1 offset:1024 sc1\n\tglobal_load_lds_dwordx4 %0, %1 offset:2048 sc1\n\tglobal_load_lds_dwordx4 %0, %1 offset:3072 sc1" ::"v"(v), "s"(sb), "s"(lds) : "memory");
}
template <int OFF> DI void lds_tr(u32x2& d, unsigned a) { asm volatile("ds_read_b64_tr_b16 %0, %1 offset:%2" : "=v"(d) : "v"(a), "i"(OFF) : "memory"); }
template <int O0, int O1> DI void lds_tr2(u32x4& d, unsigned a) {
  u32x2 lo, hi;
  asm volatile("ds_read_b64_tr_b16 %0, %2 offset:%3\n\tds_read_b64_tr_b16 %1, %2 offset:%4" : "=&v"(lo), "=&v"(hi) : "v"(a), "i"(O0), "i"(O1) : "memory");
  d = u32x4{lo[0], lo[1], hi[0], hi[1]};
}
template <int OFF> DI void lds_128(u32x4& d, unsigned a) { asm volatile("ds_read_b128 %0, %1 offset:%2" : "=v"(d) : "v"(a), "i"(OFF) : "memory"); }
template <int OFF> DI void lds_128f(f32x4& d, unsigned a) { asm volatile("ds_read_b128 %0, %1 offset:%2" : "=v"(d) : "v"(a), "i"(OFF) : "memory"); }
template <class T> DI void tie1(T& x) { asm volatile("" : "+v"(x)); }
template <class... T> DI void tie(T&... x) { (tie1(x), ...); }
template <class T> DI void tie1a(T& x) { asm volatile("" : "+a"(x)); }
template <class... T> DI void tiea(T&... x) { (tie1a(x), ...); }

constexpr int K0_MODWG = 2 * 24 * MSPLIT;
constexpr int K0_LNWG = 2 * NTILE;
constexpr int K0_PFWG = 72;
constexpr int K0_GRID = K0_MODWG + K0_LNWG + K0_PFWG;
template <int NT, int ROWS, bool DEV>
DI void mod_slab(const unsigned short* __restrict__ W, const unsigned short* __restrict__ cvec, int row0, int col0, float* __restrict__ out, float* sm) {
  constexpr int RG = NT / 8, NLD = ROWS / RG;
  static_assert(RG % 16 == 0 && ROWS % RG == 0 && ROWS / 2 <= NT, "");
  const int tid = threadIdx.x, cg = tid & 7, rg = tid >> 3;
  unsigned c0 = 0, c1 = 0;
  if (tid < ROWS / 2) { c0 = *(const unsigned*)(cvec + row0 + 2 * tid); c1 = *(const unsigned*)(cvec + D + row0 + 2 * tid); }
  const unsigned short* wp = W + (size_t)(row0 + rg) * (6 * D) + col0 + cg * 8;
  u32x4 w[NLD];
#pragma unroll
  for (int i = 0; i < NLD; ++i) w[i] = __builtin_nontemporal_load((const u32x4*)(wp + (size_t)i * RG * (6 * D)));
  float* sc = sm + RG * 128 + 64;
  if (tid < ROWS / 2) {
    auto silu = [](float v) { return v / (1.f + __expf(-v)); };
    const unsigned s0 = pk_bf16(silu(bf_lo(c0)), silu(bf_hi(c0))), s1 = pk_bf16(silu(bf_lo(c1)), silu(bf_hi(c1)));
    *(f32x2*)(sc + 2 * tid) = f32x2{bf_lo(s0), bf_hi(s0)};
    *(f32x2*)(sc + ROWS + 2 * tid) = f32x2{bf_lo(s1), bf_hi(s1)};
  }
  __syncthreads();
  float a0[8], a1[8];
#pragma unroll
  for (int j = 0; j < 8; ++j) { a0[j] = 0.f; a1[j] = 0.f; }
#pragma unroll
  for (int i = 0; i < NLD; ++i) {
    const float s0 = sc[rg + RG * i], s1 = sc[ROWS + rg + RG * i];
#pragma unroll
    for (int j = 0; j < 4; ++j) {
      const float lo = bf_lo(w[i][j]), hi = bf_hi(w[i][j]);
      a0[2 * j] = fmaf(s0, lo, a0[2 * j]); a0[2 * j + 1] = fmaf(s0, hi, a0[2 * j + 1]);
      a1[2 * j] = fmaf(s1, lo, a1[2 * j]); a1[2 * j + 1] = fmaf(s1, hi, a1[2 * j + 1]);
    }
  }
  f32x4* rp = (f32x4*)(sm + rg * 128 + (rg >= RG / 2 ? 16 : 0) + cg * 8);
  rp[0] = f32x4{a0[0], a0[1], a0[2], a0[3]}; rp[1] = f32x4{a0[4], a0[5], a0[6], a0[7]};
  rp[16] = f32x4{a1[0], a1[1], a1[2], a1[3]}; rp[17] = f32x4{a1[4], a1[5], a1[6], a1[7]};
  __syncthreads();
  if (tid < 256) {
    const int o = tid >> 1, hf = tid & 1;
    float s = 0.f;
#pragma unroll
    for (int r = 0; r < RG / 2; ++r) s += sm[(hf * (RG / 2) + r) * 128 + hf * 16 + o];
    s += __shfl_xor(s, 1);
    if (!hf) {
      if constexpr (DEV) __hip_atomic_store((GAS float*)out + (o >> 6) * 2304 + (o & 63), s, __ATOMIC_RELAXED, __HIP_MEMORY_SCOPE_AGENT);
      else out[(o >> 6) * 2304 + (o & 63)] = s;
    }
  }
}
DI float x16_sum(float v) { auto r = __builtin_amdgcn_permlane16_swap(__float_as_uint(v), __float_as_uint(v), false, false); return __uint_as_float(r[0]) + __uint_as_float(r[1]); }
__global__ __launch_bounds__(256) void k0_prep(const unsigned short* __restrict__ x_img, const unsigned short* __restrict__ x_txt,
                                               const unsigned short* __restrict__ cvec, const unsigned short* __restrict__ wmod_img,
                                               const unsigned short* __restrict__ wmod_txt, const char* __restrict__ wqkv_img,
                                               const char* __restrict__ wqkv_txt, char* __restrict__ ws) {
  __shared__ __attribute__((aligned(16))) float sm[4096 + 64 + 384];
  const int tid = threadIdx.x;
  int bid = blockIdx.x;
  RT_IN(0); STAMP_DECL; STAMP(0);
  if (bid < K0_MODWG) {
    const int ksp = bid & 3, type = (bid >> 2) & 1, slab = bid >> 3;
    mod_slab<256, D / MSPLIT, false>(type ? wmod_txt : wmod_img, cvec, ksp * (D / MSPLIT), slab * 64,
                              (float*)(ws + WS_MOD) + (size_t)(type * MSPLIT + ksp) * 2 * 2304 + slab * 64, sm);
    if (bid == 0 && tid < F_N) *(unsigned*)(ws + WS_FLAG + tid * 256) = 0u;
    STAMP(5); WAIT_VM(0); STAMP(6); STAMP_OUT(0);
  } else if (bid >= 192 + K0_LNWG) {
    const int pw = bid - (192 + K0_LNWG);
    const char* src = ((pw & 1) ? wqkv_txt : wqkv_img) + (size_t)(pw >> 1) * 24576 + tid * 16;
    u32x4 v[6];
#pragma unroll
    for (int i = 0; i < 6; ++i) v[i] = *(const u32x4*)(src + i * 4096);
#pragma unroll
    for (int i = 0; i < 6; ++i) asm volatile("" ::"v"(v[i]));
  } else {
    const int ht = bid - 192, tt = ht >> 1, half = ht & 1, b = tt >= TPB, i = tt - b * TPB;
    const int w = tid >> 6, lane = tid & 63, r = lane & 15, h = (lane >> 4) & 1, kk = lane >> 5;
    const unsigned short* xr = (i < 32) ? x_img + ((size_t)b * NI + i * 32 + half * 16 + r) * D : x_txt + ((size_t)b * NTX + half * 16 + r) * D;
    u32x4 v[6];
#pragma unroll
    for (int s = 0; s < 6; ++s) v[s] = *(const u32x4*)(xr + (12 * w + 2 * s + kk) * 16 + 8 * h);
    STAMP(11);
    float sum = 0.f;
#pragma unroll
    for (int s = 0; s < 6; ++s)
#pragma unroll
      for (int j = 0; j < 4; ++j) sum += bf_lo(v[s][j]) + bf_hi(v[s][j]);
    const float mw = xhalf_sum(x16_sum(sum)) * (1.f / 192.f);
    STAMP(12);
    float sq = 0.f;
#pragma unroll
    for (int s = 0; s < 6; ++s)
#pragma unroll
      for (int j = 0; j < 4; ++j) { const float a = bf_lo(v[s][j]) - mw, c = bf_hi(v[s][j]) - mw; sq = fmaf(a, a, sq); sq = fmaf(c, c, sq); }
    sq = xhalf_sum(x16_sum(sq));
    if (lane < 16) *(f32x2*)(sm + (r * 4 + w) * 2) = f32x2{mw, sq};
    __syncthreads();
    const f32x4 p0 = *(const f32x4*)(sm + r * 8), p1 = *(const f32x4*)(sm + r * 8 + 4);
    const float mean = (p0[0] + p0[2] + p1[0] + p1[2]) * 0.25f;
    const float d0 = p0[0] - mean, d1 = p0[2] - mean, d2 = p1[0] - mean, d3 = p1[2] - mean;
    const float var = (p0[1] + p0[3] + p1[1] + p1[3] + 192.f * (d0 * d0 + d1 * d1 + d2 * d2 + d3 * d3)) * (1.f / D);
    const float rstd = 1.f / sqrtf(var + 1e-5f);
    char* dst = ws + WS_XN + (size_t)tt * TILE_B + (size_t)(12 * w + kk) * 1024 + (h * 32 + half * 16 + r) * 16;
#pragma unroll
    for (int s = 0; s < 6; ++s) {
      u32x4 o;
#pragma unroll
      for (int j = 0; j < 4; ++j) o[j] = pk_bf16((bf_lo(v[s][j]) - mean) * rstd, (bf_hi(v[s][j]) - mean) * rstd);
      gst16(dst + s * 2048, o);
    }
    STAMP(13); WAIT_VM(0); STAMP(14); STAMP_OUT(0);
  }
  RT_OUT(0);
}

#define BAR() __builtin_amdgcn_s_barrier()
#define WAIT_LG(n) asm volatile("s_waitcnt lgkmcnt(%0)" ::"n"(n) : "memory")
template <class T> DI T sel(bool c, T a, T b) { return c ? a : b; }
constexpr int imin(int a, int b) { return a < b ? a : b; }
constexpr int imax(int a, int b) { return a > b ? a : b; }
constexpr int NSTG = 24;
constexpr int LDS_MAIN = 163840;
constexpr int ld_cnt(int k, int ns, int n) { return k == 0 ? 3 : imin(imin(imin(n, k + 12), ld_cnt(k - 1, ns, n) + 2), imax(ns, k - 1 + ns - 1)); }
DI void bars(int n) {
#pragma unroll 1
  for (int i = 0; i < n; ++i) BAR();
}
template <int N, int NS, int PER, bool PRE, class F> DI void ring_loader(F&& issue) {
  sfor<0, N>([&](auto K) {
    constexpr int k = decltype(K)::v;
    sfor<(k == 0 ? 0 : ld_cnt(k - 1, NS, N)), ld_cnt(k, NS, N)>(issue);
    if constexpr (PRE && k == 0) BAR();
    WAIT_VM(PER * (ld_cnt(k, NS, N) - k - 1)); BAR();
  });
}
constexpr int ld2_cnt(int k, int ns, int n) { return k == 0 ? 6 : imin(imin(n, ld2_cnt(k - 1, ns, n) + 4), imax(ns, k - 1 + ns - 1)); }
template <int N, int NS, int PER, int PAR, class F> DI void ring_loader2(F&& issue, bool pre) {
  sfor<0, N>([&](auto K) {
    constexpr int k = decltype(K)::v, c = ld2_cnt(k, NS, N);
    sfor<(k == 0 ? 0 : ld2_cnt(k - 1, NS, N)), c>([&](auto G) { if constexpr ((decltype(G)::v & 1) == PAR) issue(G); });
    if constexpr (k == 0) { if (pre) BAR(); }
    WAIT_VM(PER * ((c + 1 - PAR) / 2 - (k + 2 - PAR) / 2)); BAR();
  });
}
DI void glds2x2(unsigned v, const char* sb0, const char* sb1m  , unsigned lds) {
  asm volatile(
      "s_mov_b32 m0, %3\n\ts_nop 0\n\t"
      "global_load_lds_dwordx4 %0, %1 sc1\n\t"
      "global_load_lds_dwordx4 %0, %1 offset:1024 sc1\n\t"
      "global_load_lds_dwordx4 %0, %2 offset:2048 sc1\n\t"
      "global_load_lds_dwordx4 %0, %2 offset:3072 sc1"
      ::"v"(v), "s"(sb0), "s"(sb1m), "s"(lds) : "memory");
}
DI void flag_add(char* ws, int f) { __hip_atomic_fetch_add((GAS unsigned*)(ws + WS_FLAG + f * 256), 1u, __ATOMIC_RELAXED, __HIP_MEMORY_SCOPE_AGENT); }
template <bool FUSED, int NAP = 3> DI void flag_wait(char* ws, int f  , unsigned want  ) {
  if constexpr (FUSED) {
    GAS unsigned* p = (GAS unsigned*)(ws + WS_FLAG + f * 256);
#pragma unroll 1
    for (unsigned spins = 0; spins < (1u << 20); ++spins) {
      if (__all(__hip_atomic_load(p, __ATOMIC_RELAXED, __HIP_MEMORY_SCOPE_AGENT) >= want)) break;
      __builtin_amdgcn_s_sleep(NAP);
    }
    asm volatile("" ::: "memory");
  }
}

DI void w_voff(unsigned (&voff)[5], int lane, unsigned pitch) {
#pragma unroll
  for (int i = 0; i < 5; ++i) {
    const unsigned o = i * 64 + lane, row = o / 20, cb = (o % 20) * 16;
    voff[i] = row * pitch + cb - (i < 4 ? i * 1024 : 0);
  }
}

struct Args {
  const char *x_img, *x_txt, *wqkv_img, *wqkv_txt, *wo_img, *wo_txt;
  const unsigned short *cvec, *wmod_img, *wmod_txt;
  char *ws, *out;
  unsigned task[256];
};

constexpr int W_SLOT = 10240, P1_NW = 10, P1_NH = 6, HD = 4, PF_START = 10;
constexpr int P1_H = P1_NW * W_SLOT;
constexpr int P1_MV = P1_H + P1_NH * 8192;
DI void phase1(const Args& A, char* smem, int wave, int lane STAMP_ARG) {
  char* ws = A.ws;
  const int bid = blockIdx.x, xcd = bid & 7, idx = bid >> 3;
  const int rowhalf = xcd & 1, colq = xcd >> 1, h = lane >> 5;
  const bool txt = (colq == 3) & (idx >= 24);
  const int ct = sel(txt, rowhalf * 8 + (idx - 24), colq * 4 + (idx >> 3));
  const int n0 = sel(ct < 14, ct * 160, 2144);
  const unsigned lds0 = (unsigned)(size_t)(LAS char*)smem;
  const unsigned l16 = lane * 16;
  const int j = wave & 3;
  const char* xb = ws + WS_XN + (unsigned)(sel(txt, 32, rowhalf * TPB + (idx & 7) * 4) * TILE_B + j * 1024);
  const char* W = sel(txt, A.wqkv_txt, A.wqkv_img) + n0 * 2;
  const unsigned mr = imin(threadIdx.x, 383);
  const char* mp = ws + WS_MOD + (unsigned)(sel(txt, MSPLIT * 2, rowhalf) * 2304 * 4);
  asm volatile("s_nop 4" ::"s"(xb), "s"(W), "s"(mp) : "memory");
  if (ct >= 15) return;
  u32x4 m[MSPLIT];
  auto ldm = [&](const char* q) {
    const unsigned src = (mr < 192 ? 192 + mr : mr - 192) * 16;
    sfor<0, MSPLIT>([&](auto K) { constexpr int k = decltype(K)::v; gld16<0>(m[k], src, q + k * 18432); });
  };
  auto stm1 = [&](int bb) {
    tie(m[0], m[1], m[2], m[3]);
    auto f = [](u32x4 v) { return __builtin_bit_cast(f32x4, v); };
    *(f32x4*)(smem + P1_MV + bb * 6144 + mr * 16) = (f(m[0]) + f(m[1])) + (f(m[2]) + f(m[3])) + (mr < 192 ? 1.f : 0.f);
  };
  auto stm = [&]() {
    if (wave < 6) {
      stm1(0);
      if (txt) { ldm(mp + 2304 * 4); WAIT_VM(0); stm1(1); }
    }
    WAIT_LG(0);
    BAR();
  };
  if (__builtin_expect(wave >= 8, 1)) {
    if (wave >= 10) {
      u32x4 junk = {0, 0, 0, 0};
      const char* wo = sel((bool)(bid & 1), A.wo_txt, A.wo_img) + (bid >> 1) * 9216;
      const char* wg = (const char*)sel((bool)(bid & 1), A.wmod_txt, A.wmod_img) + (unsigned)((bid >> 1) * 6 * 9216 + 3072);
      const char* src = uni(sel(wave == 10, wo, wg));
      bars(PF_START);
#pragma unroll 1
      for (int i = 0; i < 9; ++i) {
        const unsigned o = i * 64 + lane, row = o / 96;
        touch16<0>(junk, sel(wave == 10, i * 1024 + l16, row * 9216 + (o - row * 96) * 16), src);
        BAR();
      }
      bars(NSTG + 1 - 9 - PF_START);
      WAIT_VM(0); tie(junk);
    } else {
      unsigned voff[5];
      w_voff(voff, lane, 4608);
      const char* p = W + (wave - 8) * 16 * 4608;
      const unsigned lds = lds0 + (wave - 8) * 5120;
      ring_loader<NSTG, P1_NW, 5, true>([&](auto G) { glds5(voff, p, lds + (decltype(G)::v % P1_NW) * W_SLOT); p += 32 * 4608; });
    }
  } else if (wave >= 4) {
    const unsigned x1 = l16 + sel(txt, TPB, 1) * TILE_B, x2 = l16 + sel(txt, 0, 2) * TILE_B, x3 = l16 + sel(txt, TPB, 3) * TILE_B;
    u32x4 xr[12][4];
    auto ldx = [&](auto I, auto U) {
      constexpr int i = decltype(I)::v, u = decltype(U)::v;
      if constexpr (i < 12) { gld16<0>(xr[i][u], u ? x1 : l16, xb + i * 4096); gld16<0>(xr[i][u + 2], u ? x3 : x2, xb + i * 4096); }
    };
    if (wave < 6) ldm(mp);
    sfor<0, HD>([&](auto I) { ldx(I, IC<0>{}); ldx(I, IC<1>{}); });
    WAIT_VM(4 * HD);
    stm();
    const unsigned mva0 = lds0 + P1_MV + h * 32 + j * 64, mva1 = mva0 + sel(txt, 6144, 0);
    char* hw = smem + P1_H + ((j >> 1) * 2 + (j & 1)) * 4096 + l16;
    f32x4 mv[2][4];
    auto rdm = [&](auto I, auto U) {
      constexpr int i = decltype(I)::v, u = decltype(U)::v;
      const unsigned a = u ? mva1 : mva0;
      if constexpr (i < 12) { lds_128f<i * 256>(mv[u][0], a); lds_128f<i * 256 + 16>(mv[u][1], a); lds_128f<3072 + i * 256>(mv[u][2], a); lds_128f<3072 + i * 256 + 16>(mv[u][3], a); }
    };
    auto modw = [&](auto I, auto T) {
      constexpr int i = decltype(I)::v, t = decltype(T)::v, q = t & 1;
      const u32x4 x = xr[i][t];
      u32x4 o;
#pragma unroll
      for (int e = 0; e < 4; ++e)
        o[e] = mod_pair(x[e], mv[q][e >> 1][(e & 1) * 2], mv[q][2 + (e >> 1)][(e & 1) * 2], mv[q][e >> 1][(e & 1) * 2 + 1], mv[q][2 + (e >> 1)][(e & 1) * 2 + 1]);
      *(u32x4*)(hw + ((2 * i) % P1_NH) * 8192 + t * 1024) = o;
    };
    rdm(IC<0>{}, IC<0>{}); rdm(IC<0>{}, IC<1>{});
    sfor<0, 12>([&](auto I) {
      constexpr int i = decltype(I)::v;
      constexpr int ahead = imin(HD - 1, 11 - i), mvn = i < 11 ? 4 : 0;
      WAIT_VM(4 * ahead + 2); WAIT_LG(i ? 6 : 4);
      modw(I, IC<0>{}); modw(I, IC<2>{});
      ldx(IC<i + HD>{}, IC<0>{});
      rdm(IC<i + 1>{}, IC<0>{});
      if constexpr (i > 0) { WAIT_LG(2 + mvn); if (i > 1 || j >= 2) BAR(); }
      else WAIT_LG(2 + mvn);
      WAIT_VM(4 * ahead + (i + HD < 12 ? 2 : 0));
      modw(I, IC<1>{}); modw(I, IC<3>{});
      ldx(IC<i + HD>{}, IC<1>{});
      if constexpr (i == 0) WAIT_LG(0);
      rdm(IC<i + 1>{}, IC<1>{});
      WAIT_LG(2 + mvn);
      BAR();
    });
    WAIT_LG(0);
    BAR();
    if (j < 2) BAR();
  } else {
    ldm(mp); WAIT_VM(0); stm();
    STAMP(2);
    f32x16 acc[5];
    const int blk = (lane >> 4) & 1, q4 = (lane & 15) >> 2, p = lane & 3;
    unsigned rw = lds0 + (8 * h + q4) * 320 + blk * 32 + p * 8, rw1 = rw + 5 * W_SLOT;
    unsigned rh = lds0 + P1_H + wave * 1024 + l16;
    tie(rw, rw1, rh);
    u32x4 wf[2][5], hh[2];
    auto rdw = [&](auto S, auto C) {
      constexpr int s = decltype(S)::v, c = decltype(C)::v, cur = s & 1, slot = (s >> 1) % P1_NW, so = (slot % 5) * W_SLOT + (s & 1) * 5120;
      if constexpr (s < KS) {
        if constexpr (c == 0) lds_128<((s >> 1) % P1_NH) * 8192 + (s & 1) * 4096>(hh[cur], rh);
        lds_tr2<so + c * 64, so + c * 64 + 1280>(wf[cur][c], slot < 5 ? rw : rw1);
      }
    };
    STAMP(3);
    BAR();
    STAMP(4);
    sfor<0, 5>([&](auto C) { rdw(IC<0>{}, C); });
    sfor<0, KS>([&](auto S) {
      constexpr int s = decltype(S)::v, cur = s & 1;
      constexpr bool more = s + 1 < KS;
      if constexpr ((s & 1) && more) BAR();
      sfor<0, 5>([&](auto C) {
        constexpr int c = decltype(C)::v;
        WAIT_LG((4 - c) * 2 + (more ? 2 * c + (c > 0) : 0));
        mfma_v<s == 0>(acc[c], wf[cur][c], hh[cur]);
        rdw(IC<s + 1>{}, C);
      });
    });
    asm volatile("s_nop 15" : "+v"(acc[0]), "+v"(acc[1]), "+v"(acc[2]), "+v"(acc[3]), "+v"(acc[4]));
    STAMP(7);
    const int tt = sel(txt, (wave & 1) * TPB + 32, rowhalf * TPB + (idx & 7) * 4 + wave);
    if (!(txt && wave >= 2)) {
#pragma unroll
      for (int c = 0; c < 5; ++c) {
        const int f0 = n0 + c * 32;
        if (ct == 14 && c < 3) continue;
        const int mat = f0 / D, st = (f0 % D) >> 4;
        char* dst = ws + WS_QKV + ((size_t)mat * NTILE + tt) * TILE_B + st * 1024 + l16;
        gst16(dst, pack8(acc[c], 0));
        gst16(dst + 1024, pack8(acc[c], 1));
      }
    }
    WAIT_VM(0);
    STAMP(8);
  }
  BAR();
  if (threadIdx.x == 0) {
    const int rb = sel(txt, 16, rowhalf * 8 + (idx & 7)), m0 = (ct * 160) / D, m1 = imin(ct * 160 + 159, 2303) / D;
    flag_add(ws, F_QKV + rb * 3 + m0);
    if (m1 != m0) flag_add(ws, F_QKV + rb * 3 + m1);
  }
}

constexpr int P2_NS = 14, P2_RA = 10;
template <int MODE>
DI void p2_consumer(f32x16 (&acc)[5], const char* priv, unsigned lds0, int lane STAMP_ARG) {
  const int h = lane >> 5, blk = (lane >> 4) & 1, q4 = (lane & 15) >> 2, p = lane & 3;
  const unsigned l16 = lane * 16;
  constexpr int PW = MODE == 2 ? 1 : 2;
  u32x4 xr[KS], wf[2][5];
  auto ldx = [&](auto S) { constexpr int s = decltype(S)::v; if constexpr (s < KS) gld16_sc1<(s & 3) * 1024>(xr[s], l16, priv + (s >> 2) * 4096); };
  sfor<0, P2_RA>(ldx);
  unsigned rw = MODE == 1 ? lds0 + (4 * h + q4) * 320 + blk * 32 + p * 8 : lds0 + l16, rw1 = rw + 5 * W_SLOT, rw2 = rw + 10 * W_SLOT;
  tie(rw, rw1, rw2);
  auto rdw = [&](auto S, auto C) {
    constexpr int s = decltype(S)::v, c = decltype(C)::v, cur = s & 1, slot = (s >> 1) % P2_NS, so = (slot % 5) * W_SLOT + (s & 1) * 5120;
    if constexpr (s < KS) {
      const unsigned a = slot < 5 ? rw : (slot < 10 ? rw1 : rw2);
      if constexpr (MODE == 2) lds_128<so + c * 1024>(wf[cur][c], a);
      else lds_tr2<so + c * 64, so + c * 64 + 2560>(wf[cur][c], a);
    }
  };
  STAMP(11);
  BAR();
  STAMP(12);
  sfor<0, 5>([&](auto C) { rdw(IC<0>{}, C); });
  sfor<0, KS>([&](auto S) {
    constexpr int s = decltype(S)::v, cur = s & 1;
    constexpr bool more = s + 1 < KS;
    if constexpr ((s & 1) && more) BAR();
    WAIT_VM(imin(P2_RA - 1, KS - 1 - s));
    sfor<0, 5>([&](auto C) {
      constexpr int c = decltype(C)::v;
      WAIT_LG((4 - c) * PW + (more ? c * PW : 0));
      if constexpr (MODE == 1) mfma_v<s == 0>(acc[c], xr[s], wf[cur][c]);
      else mfma_v<s == 0>(acc[c], wf[cur][c], xr[s]);
      rdw(IC<s + 1>{}, C);
      if constexpr (c == 2) ldx(IC<s + P2_RA>{});
    });
  });
  asm volatile("s_nop 15" : "+v"(acc[0]), "+v"(acc[1]), "+v"(acc[2]), "+v"(acc[3]), "+v"(acc[4]));
  STAMP(15);
}
template <int NT> DI void p2_softmax(f32x16 (&acc)[5], char* dst  , float* st, int own0, int lane) {
  float m = -3.0e38f;
#pragma unroll
  for (int c = 0; c < NT; ++c)
    if (c >= own0) {
#pragma unroll
      for (int r = 0; r < 16; ++r) m = fmaxf(m, acc[c][r]);
    }
  m = xhalf_max(m);
  const float mc = -m * SM_C;
  float l = 0.f;
#pragma unroll
  for (int c = 0; c < NT; ++c)
    if (c >= own0) {
#pragma unroll
      for (int r = 0; r < 16; ++r) { const float pv = __builtin_amdgcn_exp2f(fmaf(acc[c][r], SM_C, mc)); acc[c][r] = pv; l += pv; }
      gst16(dst + c * 2048, pack8(acc[c], 0));
      gst16(dst + c * 2048 + 1024, pack8(acc[c], 1));
    }
  l = xhalf_sum(l);
  __hip_atomic_store((GAS float*)st + lane, lane < 32 ? m : l, __ATOMIC_RELAXED, __HIP_MEMORY_SCOPE_AGENT);
}
template <bool FUSED> DI void phase2(const Args& A, unsigned task, char* smem, int wave, int lane STAMP_ARG) {
  char* ws = A.ws;
  const int b = (blockIdx.x >> 2) & 1, kind = task & 7;
  const int pb = (task >> 3) & 15;
  const int sc = (task >> 7) & 15;
  const unsigned lds0 = (unsigned)(size_t)(LAS char*)smem;
  const unsigned l16 = lane * 16;
  const bool isS = kind == 1;
  const int kt0 = sel(sc < 6, sc * 5, 28);
  const int n0 = sel(sc < 4, sc * 160, 608);
  if (kind == 0) return;
  if (kind == 4) {
    mod_slab<768, D, true>(pb ? A.wmod_txt : A.wmod_img, A.cvec, 0, 1536 + sc * 64, (float*)(ws + WS_MOD) + (size_t)pb * MSPLIT * 2 * 2304 + 1536 + sc * 64, (float*)smem);
    WAIT_VM(0);
    BAR();
    STAMP(17);
    if (threadIdx.x == 0) flag_add(ws, F_GATE + pb * 12 + sc);
    return;
  }
  if (kind == 3) {
    const int qt = b * TPB + 32, own0 = sc < 6 ? 0 : 2;
    float* xm = (float*)(smem + TILE_B);
    if (wave == 5) flag_wait<FUSED>(ws, sel(lane & 2, F_QKV + 16 * 3, F_QKV + 1 + 3 * sel(lane & 1, sc < 6 ? b * 8 + ((kt0 + 4) >> 2) : 16, b * 8 + (kt0 >> 2))), sel(lane & 2, 5u, 6u));
    BAR();
    if (wave >= 8) {
      const char* q = uni(ws + WS_QKV + (unsigned)(qt * TILE_B + (wave - 8) * 12288));
      asm volatile("s_nop 4" ::"s"(q) : "memory");
      sfor<0, 3>([&](auto I) { constexpr int i = decltype(I)::v; gldsN<4>(l16, q + i * 4096, lds0 + (wave - 8) * 12288 + i * 4096); });
      WAIT_VM(0);
      bars(3);
    } else if (wave >= 5) bars(3);
    else {
      const int kt = kt0 + wave;
      const char* kp = uni(ws + WS_QKV + (unsigned)((NTILE + b * TPB + kt) * TILE_B));
      asm volatile("s_nop 4" ::"s"(kp) : "memory");
      constexpr int RA = 24;
      u32x4 kf[KS], qf[KS];
      auto ldk = [&](auto S) { constexpr int s = decltype(S)::v; if constexpr (s < KS) gld16_sc1<(s & 3) * 1024>(kf[s], l16, kp + (s >> 2) * 4096); };
      auto rdq = [&](auto S) { constexpr int s = decltype(S)::v; if constexpr (s < KS) lds_128<s * 1024>(qf[s], lds0 + l16); };
      sfor<0, RA>(ldk);
      BAR();
      sfor<0, 4>(rdq);
      f32x16 acc;
      sfor<0, KS>([&](auto S) {
        constexpr int s = decltype(S)::v;
        WAIT_VM(imin(RA - 1, KS - 1 - s)); WAIT_LG(imin(3, KS - 1 - s));
        mfma_v<s == 0>(acc, kf[s], qf[s]);
        rdq(IC<s + 4>{}); ldk(IC<s + RA>{});
      });
      asm volatile("s_nop 15" : "+v"(acc));
      float m = acc[0];
#pragma unroll
      for (int r = 1; r < 16; ++r) m = fmaxf(m, acc[r]);
      m = xhalf_max(m);
      xm[wave * 32 + (lane & 31)] = wave >= own0 ? m : -3.0e38f;
      WAIT_LG(0);
      BAR();
#pragma unroll
      for (int w = 0; w < 5; ++w) m = w ? fmaxf(m, xm[w * 32 + (lane & 31)]) : xm[lane & 31];
      const float mc = -m * SM_C;
      float l = 0.f;
#pragma unroll
      for (int r = 0; r < 16; ++r) { const float pv = __builtin_amdgcn_exp2f(fmaf(acc[r], SM_C, mc)); acc[r] = pv; l += pv; }
      if (wave >= own0) {
        char* dst = ws + WS_P + (size_t)qt * PT_B + (size_t)kt * 2048 + l16;
        gst16(dst, pack8(acc, 0)); gst16(dst + 1024, pack8(acc, 1));
      }
      l = xhalf_sum(l);
      xm[160 + wave * 32 + (lane & 31)] = wave >= own0 ? l : 0.f;
      WAIT_LG(0);
      BAR();
      if (wave == 0) {
        l = 0.f;
#pragma unroll
        for (int w = 0; w < 5; ++w) l += xm[160 + w * 32 + (lane & 31)];
        __hip_atomic_store((GAS float*)(ws + WS_ST) + ((size_t)qt * NCH + sc) * 64 + lane, lane < 32 ? m : l, __ATOMIC_RELAXED, __HIP_MEMORY_SCOPE_AGENT);
      }
      WAIT_VM(0);
    }
    BAR();
    STAMP(16);
    if (threadIdx.x == 0) flag_add(ws, F_P + b * 9 + 8);
    return;
  }
  if (__builtin_expect(wave >= 4, 1)) {
    if (wave >= 8) {
      if (wave == 8) flag_wait<FUSED>(ws, sel((lane & 2) || !isS, F_QKV + sel(pb < 8, b * 8 + pb, 16) * 3 + sel(isS, 0, 2),
                                              F_QKV + 1 + 3 * sel(lane & 1, sc < 6 ? b * 8 + ((kt0 + 4) >> 2) : 16, b * 8 + (kt0 >> 2))), sel((lane & 2) && isS, 5u, 6u));
      bars(NSTG + 1);
    } else {
      unsigned voff[5];
      w_voff(voff, lane, 1536);
      if (isS) {
#pragma unroll
        for (int i = 0; i < 5; ++i) voff[i] = i * TILE_B + l16 - (i < 4 ? i * 1024 : 0);
      }
      const unsigned stride = __builtin_amdgcn_readfirstlane(sel(isS, 2048, 32 * 1536)), lds = lds0 + (wave & 1) * 5120;
      const char* p = uni(sel(isS, (const char*)ws + WS_QKV + (unsigned)((NTILE + b * TPB + kt0) * TILE_B + (wave & 1) * 1024), A.wo_img + (n0 * 2 + (wave & 1) * 16 * 1536)));
      p = sadd(p, ((wave >> 1) & 1) * stride);
      asm volatile("s_nop 4" ::"s"(p), "s"(stride) : "memory");
      auto issue = [&](auto G) { glds5(voff, p, lds + (decltype(G)::v % P2_NS) * W_SLOT); p = sadd(p, 2 * stride); };
      if (isS) BAR();
      if (wave < 6) ring_loader2<NSTG, P2_NS, 5, 0>(issue, !isS); else ring_loader2<NSTG, P2_NS, 5, 1>(issue, !isS);
    }
  } else {
    f32x16 acc[5];
    const int tl = sel(pb < 8, pb * 4 + wave, 32);
    const bool active = pb < 8 || wave == 0;
    const char* priv = uni(ws + WS_QKV + (unsigned)((sel(isS, 0, 2 * NTILE) + b * TPB + tl) * TILE_B));
    asm volatile("s_nop 4" ::"s"(priv) : "memory");
    BAR();
    if (isS) {
      const int qt = b * TPB + tl;
      p2_consumer<2>(acc, priv, lds0, lane STAMP_PASS);
      p2_softmax<5>(acc, ws + WS_P + (size_t)qt * PT_B + (size_t)kt0 * 2048 + l16, (float*)(ws + WS_ST) + ((size_t)qt * NCH + sc) * 64, sc < 6 ? 0 : 2, lane);
    } else {
      p2_consumer<1>(acc, priv, lds0, lane STAMP_PASS);
      if (active) {
        char* dst = ws + WS_VP + ((size_t)b * 24 + (n0 >> 5)) * PT_B + (size_t)tl * 2048 + l16;
#pragma unroll
        for (int c = 0; c < 5; ++c) {
          if (sc == 4 && c == 0) continue;
          gst16(dst + (size_t)c * PT_B, pack8(acc[c], 0));
          gst16(dst + (size_t)c * PT_B + 1024, pack8(acc[c], 1));
        }
      }
    }
    STAMP(16);
    WAIT_VM(0);
    STAMP(17);
  }
  BAR();
  if (threadIdx.x == 0) flag_add(ws, sel(isS, F_P + b * 9 + pb, F_VP + b * 5 + sc));
}

constexpr int P3_RA = 11;
constexpr int NSTG3 = 22;
constexpr int ld3_cnt(int k) { return k == 0 ? 2 : imin(NSTG3, k + 6); }
DI void fma4(f32x16& acc, const f32x16& t, float f, int e) {
  asm volatile("v_fmac_f32 %0, %4, %8\n\tv_fmac_f32 %1, %5, %8\n\tv_fmac_f32 %2, %6, %8\n\tv_fmac_f32 %3, %7, %8"
               : "+v"(acc[e]), "+v"(acc[e + 1]), "+v"(acc[e + 2]), "+v"(acc[e + 3]) : "v"(t[e]), "v"(t[e + 1]), "v"(t[e + 2]), "v"(t[e + 3]), "v"(f));
}
DI void chunk_scales(float (&f)[NCH], const float (&mm)[NCH], const float (&ll)[NCH]) {
  float m = mm[0];
#pragma unroll
  for (int c = 1; c < NCH; ++c) m = fmaxf(m, mm[c]);
  float l = 0.f;
#pragma unroll
  for (int c = 0; c < NCH; ++c) { f[c] = __builtin_amdgcn_exp2f((mm[c] - m) * SM_C); l = fmaf(f[c], ll[c], l); }
  const float inv = 1.f / l;
#pragma unroll
  for (int c = 0; c < NCH; ++c) f[c] *= inv;
}
DI void ld_stats(float (&mm)[NCH], float (&ll)[NCH], const char* stp, int r) {
  sfor<0, NCH>([&](auto C) { constexpr int c = decltype(C)::v; gld4<c * 256>(mm[c], r * 4, stp); gld4<c * 256 + 128>(ll[c], r * 4, stp); });
}
DI void tie_stats(float (&mm)[NCH], float (&ll)[NCH]) {
#pragma unroll
  for (int c = 0; c < NCH; ++c) tie(mm[c], ll[c]);
}
template <bool FUSED> DI void phase3(const Args& A, unsigned task, char* smem, int wave, int lane STAMP_ARG) {
  char* ws = A.ws;
  const int b = (blockIdx.x >> 2) & 1, kind = (task >> 11) & 3;
  const int r = lane & 31, h = lane >> 5;
  const unsigned lds0 = (unsigned)(size_t)(LAS char*)smem;
  const unsigned l16 = lane * 16;
  if (kind == 0) return;
  if (kind == 1) {
    const int qb = (task >> 13) & 7, dp = (task >> 16) & 31;
    if (wave >= 7) return;
    if (wave == 6) {
      flag_wait<FUSED>(ws, sel(lane & 2, sel(lane & 1, F_GATE + dp, F_P + b * 9 + qb), F_VP + b * 5 + imin((dp * 64 + (lane & 1) * 63) / 160, 4)),
                       sel(lane & 2, sel(lane & 1, 1u, 7u), 9u));
      BAR();
      return;
    }
    if (__builtin_expect(wave >= 4, 1)) {
      const char* p = uni(ws + WS_VP + (unsigned)((b * 24 + dp * 2 + (wave - 4)) * PT_B));
      const unsigned lds = lds0 + (wave - 4) * PT_B;
      asm volatile("s_nop 4" ::"s"(p) : "memory");
      BAR();
      sfor<0, NSTG3>([&](auto K) {
        constexpr int k = decltype(K)::v;
        sfor<(k == 0 ? 0 : ld3_cnt(k - 1)), ld3_cnt(k)>([&](auto G) { constexpr int g = decltype(G)::v; gldsN<3>(l16, p + g * 3072, lds + g * 3072); });
        WAIT_VM(3 * (ld3_cnt(k) - k - 1)); BAR();
      });
      return;
    }
    const int qt = b * TPB + qb * 4 + wave;
    const char* pb = uni(ws + WS_P + (unsigned)(qt * PT_B));
    const char* stp = uni(ws + WS_ST + (unsigned)(qt * NCH * 256));
    asm volatile("s_nop 4" ::"s"(pb), "s"(stp) : "memory");
    BAR();
    u32x4 pf[KSK];
    auto ldp = [&](auto S) { constexpr int s = decltype(S)::v; if constexpr (s < KSK) gld16_sc1<(s & 3) * 1024>(pf[s], l16, pb + (s >> 2) * 4096); };
    float mm[NCH], ll[NCH], f[NCH];
    sfor<0, 3>(ldp);
    ld_stats(mm, ll, stp, r);
    sfor<3, P3_RA>(ldp);
    f32x16 acc[2], tmp[2];
    acc[0] = f32x16{}; acc[1] = f32x16{};
    tie(acc[0], acc[1]);
    u32x4 vf[2][2];
    unsigned rv0 = lds0 + l16, rv1 = rv0 + PT_B;
    tie(rv0, rv1);
    auto rdv = [&](auto S, auto T) {
      constexpr int s = decltype(S)::v, t = decltype(T)::v;
      if constexpr (s < 64) lds_128<s * 1024>(vf[s & 1][t], t ? rv1 : rv0);
      else if constexpr (s < KSK) lds_128<(s - 2) * 1024>(vf[s & 1][t], (t ? rv1 : rv0) + 2048);
    };
    u32x4 xv[4];
    float gl;
    const unsigned orow = ((b * NI + (qb * 4 + wave) * 32 + (lane >> 3)) * D + dp * 64) * 2 + (lane & 7) * 16;
    STAMP(19);
    BAR();
    STAMP(20);
    rdv(IC<0>{}, IC<0>{}); rdv(IC<0>{}, IC<1>{}); rdv(IC<1>{}, IC<0>{}); rdv(IC<1>{}, IC<1>{});
    sfor<0, KSK>([&](auto S) {
      constexpr int s = decltype(S)::v, cur = s & 1, c = s / 10, cs = s % 10;
      if constexpr ((s + 2) % 3 == 0 && s + 2 < KSK) BAR();
      WAIT_VM(imin(P3_RA - 1, KSK - 1 - s) + (s < 3 ? 2 * NCH : 0) + (s > KSK - 1 - P3_RA ? 5 : 0));
      sfor<0, 2>([&](auto T) {
        constexpr int t = decltype(T)::v;
        WAIT_LG((1 - t) + (s + 1 < KSK ? 2 : 0) + (s + 2 < KSK ? t : 0));
        mfma_v<cs == 0>(tmp[t], vf[cur][t], pf[s]);
        rdv(IC<s + 2>{}, T);
        if constexpr (t == 0) {
          ldp(IC<s + P3_RA>{});
          if constexpr (s == KSK - 1 - P3_RA) {
            sfor<0, 4>([&](auto E) { constexpr int e = decltype(E)::v; gld16<0>(xv[e], orow, A.x_img + e * 8 * D * 2); });
            gld4<0>(gl, lane * 4, ws + WS_MOD + (unsigned)((b * 2304 + 1536 + dp * 64) * 4));
          }
        }
      });
      if constexpr (cs == 9) {
        asm volatile("s_nop 9" : "+v"(tmp[0]));
        fma4(acc[0], tmp[0], f[c], 0); fma4(acc[0], tmp[0], f[c], 4); fma4(acc[0], tmp[0], f[c], 8); fma4(acc[0], tmp[0], f[c], 12);
        fma4(acc[1], tmp[1], f[c], 0); fma4(acc[1], tmp[1], f[c], 4); fma4(acc[1], tmp[1], f[c], 8); fma4(acc[1], tmp[1], f[c], 12);
      }
      if constexpr (s == 4) { tie_stats(mm, ll); chunk_scales(f, mm, ll); SBAR(); }
    });
    asm volatile("s_nop 15" : "+v"(tmp[0]), "+v"(tmp[1]));
    WAIT_VM(0); tie(xv[0], xv[1], xv[2], xv[3], gl);
    BAR();
    STAMP(21);
    {
      char* tb = smem + wave * 8192;
      *(float*)(smem + 32768 + wave * 256 + lane * 4) = gl;
#pragma unroll
      for (int e = 0; e < 8; ++e) {
        const int t = e >> 2, gq = e & 3;
        f32x4 y;
#pragma unroll
        for (int k = 0; k < 4; ++k) y[k] = fmaf(tmp[t][4 * gq + k], f[6], acc[t][4 * gq + k]);
        *(f32x4*)(tb + r * 256 + (((2 * e + h) ^ (r & 15)) * 16)) = y;
      }
      const f32x4 g0 = *(const f32x4*)(smem + 32768 + wave * 256 + (lane & 7) * 32), g1 = *(const f32x4*)(smem + 32768 + wave * 256 + (lane & 7) * 32 + 16);
#pragma unroll
      for (int e = 0; e < 4; ++e) {
        const int row = e * 8 + (lane >> 3), c2 = (lane & 7) * 2;
        const f32x4 y0 = *(const f32x4*)(tb + row * 256 + ((c2 ^ (row & 15)) * 16));
        const f32x4 y1 = *(const f32x4*)(tb + row * 256 + (((c2 + 1) ^ (row & 15)) * 16));
        u32x4 o = {pk_bf16(fmaf(g0[0], y0[0], bf_lo(xv[e][0])), fmaf(g0[1], y0[1], bf_hi(xv[e][0]))), pk_bf16(fmaf(g0[2], y0[2], bf_lo(xv[e][1])), fmaf(g0[3], y0[3], bf_hi(xv[e][1]))),
                   pk_bf16(fmaf(g1[0], y1[0], bf_lo(xv[e][2])), fmaf(g1[1], y1[1], bf_hi(xv[e][2]))), pk_bf16(fmaf(g1[2], y1[2], bf_lo(xv[e][3])), fmaf(g1[3], y1[3], bf_hi(xv[e][3])))};
        *(u32x4*)(A.out + orow + (size_t)e * 8 * D * 2) = o;
      }
    }
    STAMP(22);
  } else {
    if (wave >= 8) return;
    const int T = (task >> 16) & 31;
    const int qt = b * TPB + 32;
    constexpr int LV = 0, LW = 8 * 10240, LR = LW + 768 * 64;
    if (wave == 7) {
      flag_wait<FUSED, 16>(ws, F_P + b * 9 + 8, 7u);
      flag_wait<FUSED>(ws, sel(lane < 9, F_QKV + 2 + 3 * sel(lane < 8, b * 8 + lane, 16), F_GATE + 12 + (T >> 1)), sel(lane < 9, 6u, 1u));
    }
    BAR();
    STAMP(1);
    const int g0 = sel(wave < 6, wave * 5, 28);
    const char* vsrc = uni(ws + WS_QKV + (unsigned)((2 * NTILE + b * TPB + g0) * TILE_B + T * 2048));
    const char* pA = uni(ws + WS_P + (unsigned)(qt * PT_B + g0 * 2048));
    const char* stp = uni(ws + WS_ST + (unsigned)(qt * NCH * 256));
    const char* wsrc = uni(A.wo_txt + (unsigned)(96 * wave * 1536 + T * 64));
    asm volatile("s_nop 4" ::"s"(vsrc), "s"(pA), "s"(stp), "s"(wsrc) : "memory");
    const unsigned lv = lds0 + LV + wave * 10240;
    sfor<0, 5>([&](auto K) { constexpr int kt = decltype(K)::v; gldsN<2>(l16, vsrc + kt * TILE_B, lv + kt * 2048); });
    u32x4 pa[10];
    sfor<0, 10>([&](auto S) { constexpr int s = decltype(S)::v; gld16_sc1<(s & 3) * 1024>(pa[s], l16, pA + (s >> 2) * 4096); });
    float mm[NCH], ll[NCH], f[NCH];
    ld_stats(mm, ll, stp, r);
    {
      const unsigned wv = (lane >> 2) * 1536 + (lane & 3) * 16;
      sfor<0, 6>([&](auto I) { constexpr int i = decltype(I)::v; gldsN<1>(wv, wsrc + i * 16 * 1536, lds0 + LW + (96 * wave + 16 * i) * 64); });
    }
    const int blk = (lane >> 4) & 1, q4 = (lane & 15) >> 2, p = lane & 3;
    const unsigned va = lv + blk * 1024 + ((p & 1) * 32 + 4 * h + q4) * 16 + (p >> 1) * 8;
    WAIT_VM(6);
    STAMP(10);
    tie_stats(mm, ll);
    tie(pa[0], pa[1], pa[2], pa[3], pa[4], pa[5], pa[6], pa[7], pa[8], pa[9]);
    f32x16 ta = f32x16{};
    {
      u32x4 v[10];
      auto rdv = [&](auto S) { constexpr int s = decltype(S)::v, o = (s >> 1) * 2048 + (s & 1) * 256; lds_tr2<o, o + 128>(v[s], va); };
      sfor<0, 7>(rdv);
      chunk_scales(f, mm, ll);
      const int skip = wave >= 6 ? 4 : 0;
      sfor<0, 10>([&](auto S) {
        constexpr int s = decltype(S)::v;
        WAIT_LG(2 * (imin(s + 7, 10) - s - 1)); tie(v[s]);
        if constexpr (s + 7 < 10) rdv(IC<s + 7>{});
        if (s >= skip) ta = mfma(v[s], pa[s], ta);
      });
    }
    float fa = f[0];
#pragma unroll
    for (int w = 1; w < 7; ++w) if (wave == w) fa = f[w];
    f32x4* red = (f32x4*)(smem + LR);
    if (wave < 7) {
#pragma unroll
      for (int e = 0; e < 4; ++e) red[(wave * 4 + e) * 64 + lane] = f32x4{ta[4 * e], ta[4 * e + 1], ta[4 * e + 2], ta[4 * e + 3]} * fa;
    }
    u32x2 xv; u32x4 gq;
    gld8<0>(xv, (r * D + 4 * h + (wave & 3) * 8) * 2, uni(A.x_txt + (unsigned)((b * NTX * D + T * 32) * 2)));
    gld16_sc1<0>(gq, h * 16 + (wave & 3) * 32, uni(ws + WS_MOD + (unsigned)(((MSPLIT * 2 + b) * 2304 + 1536 + T * 32) * 4)));
    WAIT_LG(0);
    BAR();
    if (wave < 2) {
      f32x4 s0 = red[(2 * wave) * 64 + lane], s1 = red[(2 * wave + 1) * 64 + lane];
#pragma unroll
      for (int w = 1; w < 7; ++w) { s0 += red[(w * 4 + 2 * wave) * 64 + lane]; s1 += red[(w * 4 + 2 * wave + 1) * 64 + lane]; }
      gst16(ws + WS_AT + (size_t)b * TILE_B + T * 2048 + wave * 1024 + l16, u32x4{pk_bf16(s0[0], s0[1]), pk_bf16(s0[2], s0[3]), pk_bf16(s1[0], s1[1]), pk_bf16(s1[2], s1[3])});
      WAIT_VM(0);
      STAMP(23);
      if (lane == 0) flag_add(ws, F_TXT + b);
    }
    u32x4 wq[6];
    WAIT_VM(0); tie(xv, gq);
    {
      const unsigned wa = lds0 + LW + (96 * wave + 4 * h + q4) * 64 + blk * 32 + p * 8;
      sfor<0, 6>([&](auto S) { constexpr int s = decltype(S)::v; lds_tr2<s * 1024, s * 1024 + 512>(wq[s], wa); });
    }
    if (wave == 7) flag_wait<true>(ws, F_TXT + b, 48u);
    WAIT_LG(0);
    BAR();
    u32x4 af[6];
    const char* ab = uni(ws + WS_AT + (unsigned)(b * TILE_B + wave * 6144));
    asm volatile("s_nop 4" ::"s"(ab) : "memory");
    sfor<0, 6>([&](auto S) { constexpr int s = decltype(S)::v; gld16_sc1<(s & 3) * 1024>(af[s], l16, ab + (s >> 2) * 4096); });
    WAIT_VM(0);
    tie(af[0], af[1], af[2], af[3], af[4], af[5], wq[0], wq[1], wq[2], wq[3], wq[4], wq[5]);
    f32x16 tb = f32x16{};
#pragma unroll
    for (int s = 0; s < 6; ++s) tb = mfma(wq[s], af[s], tb);
#pragma unroll
    for (int e = 0; e < 4; ++e) red[(wave * 4 + e) * 64 + lane] = f32x4{tb[4 * e], tb[4 * e + 1], tb[4 * e + 2], tb[4 * e + 3]};
    WAIT_LG(0);
    BAR();
    if (wave < 4) {
      f32x4 o = red[wave * 64 + lane];
#pragma unroll
      for (int w = 1; w < 8; ++w) o += red[(w * 4 + wave) * 64 + lane];
      const f32x4 gt = __builtin_bit_cast(f32x4, gq);
      u32x2 ov = {pk_bf16(fmaf(gt[0], o[0], bf_lo(xv[0])), fmaf(gt[1], o[1], bf_hi(xv[0]))), pk_bf16(fmaf(gt[2], o[2], bf_lo(xv[1])), fmaf(gt[3], o[3], bf_hi(xv[1])))};
      *(u32x2*)(A.out + ((size_t)2 * NI * D + ((size_t)b * NTX + r) * D + T * 32 + 4 * h + wave * 8) * 2) = ov;
    }
  }
}

template <int PH> __global__ __launch_bounds__(768, 3) void k_main(Args A) {
  extern __shared__ __attribute__((aligned(16))) char smem[];
  STAMP_DECL; STAMP(0); RT_IN(1);
  const int wave = __builtin_amdgcn_readfirstlane(threadIdx.x >> 6), lane = threadIdx.x & 63;
  const unsigned task = A.task[blockIdx.x];
  if constexpr (PH & 1) phase1(A, smem, wave, lane STAMP_PASS);
  STAMP(9);
  RT_MID(0);
  if constexpr (PH & 2) phase2<PH == 7>(A, task, smem, wave, lane STAMP_PASS);
  STAMP(18);
  RT_MID(1);
  if constexpr (PH & 4) phase3<PH == 7>(A, task, smem, wave, lane STAMP_PASS);
}

#define ONE_LAUNCH 1
extern "C" void kernel_launch(void* const* d_in, const int* in_sizes, int n_in, void* d_out, int out_size, void* d_ws, size_t ws_size,
                              hipStream_t stream) {
  (void)in_sizes; (void)n_in; (void)out_size; (void)ws_size;
  Args A;
  A.x_img = (const char*)d_in[0]; A.x_txt = (const char*)d_in[1]; A.cvec = (const unsigned short*)d_in[2];
  A.wqkv_img = (const char*)d_in[3]; A.wqkv_txt = (const char*)d_in[4]; A.wo_img = (const char*)d_in[5]; A.wo_txt = (const char*)d_in[6];
  A.wmod_img = (const unsigned short*)d_in[7]; A.wmod_txt = (const unsigned short*)d_in[8];
  A.ws = (char*)d_ws; A.out = (char*)d_out;
  for (int x = 0; x < 8; ++x) {
    const int sub = x & 3, qh = sub & 1, ch = sub >> 1, nq = 5 - qh, nS = nq * (4 - ch), v0 = sub == 0 ? 0 : (sub == 1 ? 7 : (sub == 2 ? 18 : 30));
    int ntx = 0, spare = 0, img = 0;
    for (int i = 0; i < 32; ++i) {
      unsigned t; bool text = false; int T = 0;
      if (i < nS) {
        const int pb = qh * 5 + i % nq, sc = ch * 4 + i / nq;
        t = (pb < 8 ? 1u : 3u) | (pb << 3) | (sc << 7);
        if (pb == 8 && ntx < 2) { text = true; T = 20 + ch * 2 + ntx++; }
      } else if (i < 27) { const int v = v0 + i - nS; t = 2u | ((v / 5) << 3) | ((v % 5) << 7); }
      else {
        const int g = (i - 27) * 8 + x;
        t = i < 30 ? (4u | ((g & 1) << 3) | ((g >> 1) << 7)) : 0u;
        text = true; T = sub * 5 + spare++;
      }
      if (text) t |= (2u << 11) | (T << 16);
      else if (img < 24) { t |= (1u << 11) | ((qh * 4 + (img & 3)) << 13) | ((ch * 6 + (img >> 2)) << 16); ++img; }
      A.task[i * 8 + x] = t;
    }
  }
  k0_prep<<<K0_GRID, 256, 0, stream>>>((const unsigned short*)A.x_img, (const unsigned short*)A.x_txt, A.cvec, A.wmod_img, A.wmod_txt, A.wqkv_img, A.wqkv_txt, A.ws);
  (void)hipFuncSetAttribute((const void*)k_main<7>, hipFuncAttributeMaxDynamicSharedMemorySize, LDS_MAIN);
  k_main<7><<<256, 768, LDS_MAIN, stream>>>(A);
}
```

```cpp
#include <hip/hip_runtime.h>
#include <hip/hip_bf16.h>
#include <stdint.h>

typedef short    bf16x8 __attribute__((ext_vector_type(8)));
typedef float    f32x16 __attribute__((ext_vector_type(16)));
typedef float    f32x4  __attribute__((ext_vector_type(4)));
typedef float    f32x2  __attribute__((ext_vector_type(2)));
typedef unsigned u32x4  __attribute__((ext_vector_type(4)));
typedef unsigned u32x2  __attribute__((ext_vector_type(2)));
typedef __bf16   bf16x2_t __attribute__((ext_vector_type(2)));

#define DI __device__ __forceinline__
#define LAS __attribute__((address_space(3)))
#define GAS __attribute__((address_space(1)))
#define SBAR() __builtin_amdgcn_sched_barrier(0)

constexpr int D = 768, NI = 1024, NTX = 32;
constexpr int TPB = 33;
constexpr int NTILE = 66;
constexpr int KS = 48;
#define PAD1 0
#define PAD2 0
constexpr int TILE_B = KS * 1024 + PAD1;
constexpr int KSK = 66;
constexpr int PT_B = KSK * 1024 + PAD2;
constexpr int NCH = 7;
constexpr int MSPLIT = 4;

constexpr size_t WS_FLAG = 0;
constexpr int F_QKV = 0, F_P = 51, F_VP = 69, F_GATE = 79, F_TXT = 103, F_N = 128;
constexpr size_t WS_MOD  = 32768;
constexpr size_t WS_XN   = WS_MOD + (size_t)2 * MSPLIT * 2 * 2304 * 4;
constexpr size_t WS_QKV  = WS_XN + (size_t)NTILE * TILE_B;
constexpr size_t WS_P    = WS_QKV + (size_t)3 * NTILE * TILE_B;
constexpr size_t WS_VP   = WS_P + (size_t)NTILE * PT_B;
constexpr size_t WS_ST   = WS_VP + (size_t)48 * PT_B;
constexpr size_t WS_AT   = WS_ST + (size_t)NTILE * NCH * 2 * 32 * 4;
constexpr size_t WS_END  = WS_AT + (size_t)2 * TILE_B;

constexpr float SM_C = 0.03608439182435161f * 1.4426950408889634f;

template <class T> DI T sel(bool c, T a, T b) { return c ? a : b; }
DI int imin0(int a, int b) { return a < b ? a : b; }
template <int I> struct IC { static constexpr int v = I; };
template <int B, int E, class F> DI void sfor(F&& f) { if constexpr (B < E) { f(IC<B>{}); sfor<B + 1, E>(f); } }

DI unsigned pk_bf16(float a, float b) { f32x2 v = {a, b}; bf16x2_t r = __builtin_convertvector(v, bf16x2_t); return __builtin_bit_cast(unsigned, r); }
DI float bf_lo(unsigned u) { return __uint_as_float(u << 16); }
DI float bf_hi(unsigned u) { return __uint_as_float(u & 0xffff0000u); }
DI f32x16 mfma(u32x4 a, u32x4 b, f32x16 c) {
  return __builtin_amdgcn_mfma_f32_32x32x16_bf16(__builtin_bit_cast(bf16x8, a), __builtin_bit_cast(bf16x8, b), c, 0, 0, 0);
}
template <bool FIRST> DI void mfma_v(f32x16& c, u32x4 a, u32x4 b) {
  if constexpr (FIRST) asm volatile("v_mfma_f32_32x32x16_bf16 %0, %1, %2, 0" : "=&v"(c) : "v"(a), "v"(b));
  else asm volatile("v_mfma_f32_32x32x16_bf16 %0, %1, %2, %0" : "+v"(c) : "v"(a), "v"(b));
}
DI float xhalf_max(float v) { auto r = __builtin_amdgcn_permlane32_swap(__float_as_uint(v), __float_as_uint(v), false, false); return fmaxf(__uint_as_float(r[0]), __uint_as_float(r[1])); }
DI float xhalf_sum(float v) { auto r = __builtin_amdgcn_permlane32_swap(__float_as_uint(v), __float_as_uint(v), false, false); return __uint_as_float(r[0]) + __uint_as_float(r[1]); }
DI u32x4 pack8(const f32x16& a, int s) {
  u32x4 r = {pk_bf16(a[8 * s], a[8 * s + 1]), pk_bf16(a[8 * s + 2], a[8 * s + 3]), pk_bf16(a[8 * s + 4], a[8 * s + 5]), pk_bf16(a[8 * s + 6], a[8 * s + 7])};
  return r;
}
DI unsigned mod_pair(unsigned x, float s0, float t0, float s1, float t1) {
  unsigned o, a, b;
  asm volatile("v_lshlrev_b32 %1, 16, %3\n\tv_and_b32 %2, 0xffff0000, %3\n\tv_fma_f32 %1, %1, %4, %5\n\tv_fma_f32 %2, %2, %6, %7\n\tv_cvt_pk_bf16_f32 %0, %1, %2"
      : "=v"(o), "=&v"(a), "=&v"(b) : "v"(x), "v"(s0), "v"(t0), "v"(s1), "v"(t1));
  return o;
}
DI const char* sadd(const char* p, unsigned d) {
  const unsigned long long u = (unsigned long long)p;
  unsigned lo, hi;
  asm("s_add_u32 %0, %2, %4\n\ts_addc_u32 %1, %3, 0" : "=&s"(lo), "=&s"(hi) : "s"((unsigned)u), "s"((unsigned)(u >> 32)), "s"(d) : "scc");
  return (const char*)(((unsigned long long)hi << 32) | lo);
}
DI const char* uni(const char* p) {
  unsigned long long u = (unsigned long long)p;
  unsigned lo = __builtin_amdgcn_readfirstlane((unsigned)u), hi = __builtin_amdgcn_readfirstlane((unsigned)(u >> 32));
  return (const char*)(((unsigned long long)hi << 32) | lo);
}

#define STAMP(i)
#define STAMP_DECL
#define STAMP_OUT(k)
#define STAMP_ARG
#define STAMP_PASS
#define RT_IN(k)
#define RT_OUT(k)
#define RT_MID(i)

#define ST_POL "sc1"
DI void gst16(char* p, u32x4 v) { asm volatile("global_store_dwordx4 %0, %1, off " ST_POL "\n\ts_nop 1" ::"v"(p), "v"(v) : "memory"); }
#define WAIT_VM(n) asm volatile("s_waitcnt vmcnt(%0)" ::"n"(n) : "memory")
#define WAIT_LG0() asm volatile("s_waitcnt lgkmcnt(0)" ::: "memory")
template <int OFF> DI void gld16(u32x4& d, unsigned voff, const char* sb) {
  asm volatile("global_load_dwordx4 %0, %1, %2 offset:%3" : "=v"(d) : "v"(voff), "s"(sb), "i"(OFF) : "memory");
}
template <int OFF> DI void gld16a(u32x4& d, unsigned voff, const char* sb) {
  asm volatile("global_load_dwordx4 %0, %1, %2 offset:%3" : "=a"(d) : "v"(voff), "s"(sb), "i"(OFF) : "memory");
}
template <int OFF> DI void gld8(u32x2& d, unsigned voff, const char* sb) {
  asm volatile("global_load_dwordx2 %0, %1, %2 offset:%3" : "=v"(d) : "v"(voff), "s"(sb), "i"(OFF) : "memory");
}
template <int OFF> DI void gld4(float& d, unsigned voff, const char* sb) {
  asm volatile("global_load_dword %0, %1, %2 offset:%3 sc1" : "=v"(d) : "v"(voff), "s"(sb), "i"(OFF) : "memory");
}
template <int OFF> DI void touch16(u32x4& junk, unsigned voff, const char* sb) {
  asm volatile("global_load_dwordx4 %0, %1, %2 offset:%3" : "+v"(junk) : "v"(voff), "s"(sb), "i"(OFF) : "memory");
}
DI void touch(unsigned& junk, unsigned voff, const char* sb) {
  asm volatile("global_load_dword %0, %1, %2" : "+v"(junk) : "v"(voff), "s"(sb) : "memory");
}
template <int OFF> DI void gld16_sc1(u32x4& d, unsigned voff, const char* sb) {
  asm volatile("global_load_dwordx4 %0, %1, %2 offset:%3 sc1" : "=v"(d) : "v"(voff), "s"(sb), "i"(OFF) : "memory");
}
#define GLDS5(NAME, POL) DI void NAME(const unsigned (&v)[5], const char* sb, unsigned lds) { \
  asm volatile("s_mov_b32 m0, %6\n\ts_nop 0\n\t" \
      "global_load_lds_dwordx4 %0, %5 " POL "\n\t" \
      "global_load_lds_dwordx4 %1, %5 offset:1024 " POL "\n\t" \
      "global_load_lds_dwordx4 %2, %5 offset:2048 " POL "\n\t" \
      "global_load_lds_dwordx4 %3, %5 offset:3072 " POL "\n\t" \
      "s_add_u32 m0, m0, 4096\n\ts_nop 0\n\t" \
      "global_load_lds_dwordx4 %4, %5 " POL \
      ::"v"(v[0]), "v"(v[1]), "v"(v[2]), "v"(v[3]), "v"(v[4]), "s"(sb), "s"(lds) : "memory", "scc"); }
#define W_POL "nt"
GLDS5(glds5, "sc1")
GLDS5(glds5w, W_POL)
template <int N> DI void gldsN(unsigned v, const char* sb, unsigned lds) {
  static_assert(N >= 1 && N <= 4, "");
  if constexpr (N == 1) asm volatile("s_mov_b32 m0, %2\n\ts_nop 0\n\tglobal_load_lds_dwordx4 %0, %1 sc1" ::"v"(v), "s"(sb), "s"(lds) : "memory");
  if constexpr (N == 2) asm volatile("s_mov_b32 m0, %2\n\ts_nop 0\n\tglobal_load_lds_dwordx4 %0, %1 sc1\n\tglobal_load_lds_dwordx4 %0, %1 offset:1024 sc1" ::"v"(v), "s"(sb), "s"(lds) : "memory");
  if constexpr (N == 3) asm volatile("s_mov_b32 m0, %2\n\ts_nop 0\n\tglobal_load_lds_dwordx4 %0, %1 sc1\n\tglobal_load_lds_dwordx4 %0, %1 offset:1024 sc1\n\tglobal_load_lds_dwordx4 %0, %1 offset:2048 sc1" ::"v"(v), "s"(sb), "s"(lds) : "memory");
  if constexpr (N == 4) asm volatile("s_mov_b32 m0, %2\n\ts_nop 0\n\tglobal_load_lds_dwordx4 %0, %1 sc1\n\tglobal_load_lds_dwordx4 %0, %1 offset:1024 sc1\n\tglobal_load_lds_dwordx4 %0, %1 offset:2048 sc1\n\tglobal_load_lds_dwordx4 %0, %1 offset:3072 sc1" ::"v"(v), "s"(sb), "s"(lds) : "memory");
}
template <int OFF> DI void lds_tr(u32x2& d, unsigned a) { asm volatile("ds_read_b64_tr_b16 %0, %1 offset:%2" : "=v"(d) : "v"(a), "i"(OFF) : "memory"); }
template <int O0, int O1> DI void lds_tr2(u32x4& d, unsigned a) {
  u32x2 lo, hi;
  asm volatile("ds_read_b64_tr_b16 %0, %2 offset:%3\n\tds_read_b64_tr_b16 %1, %2 offset:%4" : "=&v"(lo), "=&v"(hi) : "v"(a), "i"(O0), "i"(O1) : "memory");
  d = u32x4{lo[0], lo[1], hi[0], hi[1]};
}
template <int OFF> DI void lds_128(u32x4& d, unsigned a) { asm volatile("ds_read_b128 %0, %1 offset:%2" : "=v"(d) : "v"(a), "i"(OFF) : "memory"); }
template <int OFF> DI void lds_128f(f32x4& d, unsigned a) { asm volatile("ds_read_b128 %0, %1 offset:%2" : "=v"(d) : "v"(a), "i"(OFF) : "memory"); }
template <class T> DI void tie1(T& x) { asm volatile("" : "+v"(x)); }
template <class... T> DI void tie(T&... x) { (tie1(x), ...); }
template <class T> DI void tie1a(T& x) { asm volatile("" : "+a"(x)); }
template <class... T> DI void tiea(T&... x) { (tie1a(x), ...); }

constexpr int K0_MODWG = 2 * 24 * MSPLIT;
constexpr int K0_LNWG = 2 * NTILE;
constexpr int CODE_KB = 56;
constexpr int K0_PFWG = 2;
constexpr int K0_GRID = K0_LNWG + K0_MODWG / 2 + K0_PFWG;
template <int NT, int ROWS, bool DEV, bool PRE>
DI void mod_slab(const unsigned short* __restrict__ W, const unsigned short* __restrict__ cvec, int row0, int col0, float* __restrict__ out, float* sm, int tid,
                 u32x4 (&w)[ROWS * 8 / NT]  , unsigned c0, unsigned c1) {
  constexpr int RG = NT / 8, NLD = ROWS / RG;
  static_assert(RG % 16 == 0 && ROWS % RG == 0 && ROWS / 2 <= NT, "");
  const int cg = tid & 7, rg = tid >> 3;
  if constexpr (!PRE) {
    if (tid < ROWS / 2) { c0 = *(const unsigned*)(cvec + row0 + 2 * tid); c1 = *(const unsigned*)(cvec + D + row0 + 2 * tid); }
    const unsigned short* wp = W + (size_t)(row0 + rg) * (6 * D) + col0 + cg * 8;
#pragma unroll
    for (int i = 0; i < NLD; ++i) w[i] = __builtin_nontemporal_load((const u32x4*)(wp + (size_t)i * RG * (6 * D)));
  }
  float* sc = sm + RG * 128 + 64;
  if (tid < ROWS / 2) {
    auto silu = [](float v) { return v / (1.f + __expf(-v)); };
    const unsigned s0 = pk_bf16(silu(bf_lo(c0)), silu(bf_hi(c0))), s1 = pk_bf16(silu(bf_lo(c1)), silu(bf_hi(c1)));
    *(f32x2*)(sc + 2 * tid) = f32x2{bf_lo(s0), bf_hi(s0)};
    *(f32x2*)(sc + ROWS + 2 * tid) = f32x2{bf_lo(s1), bf_hi(s1)};
  }
  __syncthreads();
  float a0[8], a1[8];
#pragma unroll
  for (int j = 0; j < 8; ++j) { a0[j] = 0.f; a1[j] = 0.f; }
#pragma unroll
  for (int i = 0; i < NLD; ++i) {
    const float s0 = sc[rg + RG * i], s1 = sc[ROWS + rg + RG * i];
#pragma unroll
    for (int j = 0; j < 4; ++j) {
      const float lo = bf_lo(w[i][j]), hi = bf_hi(w[i][j]);
      a0[2 * j] = fmaf(s0, lo, a0[2 * j]); a0[2 * j + 1] = fmaf(s0, hi, a0[2 * j + 1]);
      a1[2 * j] = fmaf(s1, lo, a1[2 * j]); a1[2 * j + 1] = fmaf(s1, hi, a1[2 * j + 1]);
    }
  }
  f32x4* rp = (f32x4*)(sm + rg * 128 + (rg >= RG / 2 ? 16 : 0) + cg * 8);
  rp[0] = f32x4{a0[0], a0[1], a0[2], a0[3]}; rp[1] = f32x4{a0[4], a0[5], a0[6], a0[7]};
  rp[16] = f32x4{a1[0], a1[1], a1[2], a1[3]}; rp[17] = f32x4{a1[4], a1[5], a1[6], a1[7]};
  __syncthreads();
  if (tid < 256) {
    const int o = tid >> 1, hf = tid & 1;
    float s = 0.f;
#pragma unroll
    for (int r = 0; r < RG / 2; ++r) s += sm[(hf * (RG / 2) + r) * 128 + hf * 16 + o];
    s += __shfl_xor(s, 1);
    if (!hf) {
      if constexpr (DEV) __hip_atomic_store((GAS float*)out + (o >> 6) * 2304 + (o & 63), s, __ATOMIC_RELAXED, __HIP_MEMORY_SCOPE_AGENT);
      else out[(o >> 6) * 2304 + (o & 63)] = s;
    }
  }
}
DI float x16_sum(float v) { auto r = __builtin_amdgcn_permlane16_swap(__float_as_uint(v), __float_as_uint(v), false, false); return __uint_as_float(r[0]) + __uint_as_float(r[1]); }
__global__ __launch_bounds__(512) void k0_prep(const unsigned short* __restrict__ x_img, const unsigned short* __restrict__ x_txt,
                                               const unsigned short* __restrict__ cvec, const unsigned short* __restrict__ wmod_img,
                                               const unsigned short* __restrict__ wmod_txt, const char* __restrict__ wqkv_img,
                                               const char* __restrict__ wqkv_txt, char* __restrict__ ws) {
  __shared__ __attribute__((aligned(16))) float sm2[2][4096 + 64 + 384];
  const int bid = blockIdx.x;
  RT_IN(0); STAMP_DECL; STAMP(0);
  const bool is_ln = bid < K0_LNWG, is_mod = !is_ln & (bid < K0_LNWG + K0_MODWG / 2);
  const int tt = bid >> 1, half = bid & 1, b = tt >= TPB, i = tt - b * TPB;
  const int w = threadIdx.x >> 6, lane = threadIdx.x & 63, r = lane & 15, h = (lane >> 4) & 1, kk = lane >> 5;
  const int m = (bid - K0_LNWG) * 2 + (threadIdx.x >> 8), ksp = m & 3, type = (m >> 2) & 1, slab = m >> 3;
  const char* xa = sel(i < 32, (const char*)x_img + (unsigned)((b * NI + i * 32 + half * 16 + r) * D * 2), (const char*)x_txt + (unsigned)((b * NTX + half * 16 + r) * D * 2)) + (6 * w + kk) * 32 + 16 * h;
  const char* ma = (const char*)sel((bool)type, wmod_txt, wmod_img) + (unsigned)(((ksp * (D / MSPLIT) + ((threadIdx.x & 255) >> 3)) * (6 * D) + slab * 64 + (threadIdx.x & 7) * 8) * 2);
  const char* base0 = sel(is_mod, ma, sel(is_ln, xa, (const char*)x_img));
  const unsigned st0 = sel(is_mod, 32 * 6 * D * 2, 64);
  const int ci = ksp * (D / MSPLIT) + 2 * imin0(threadIdx.x & 255, D / MSPLIT / 2 - 1);
  const unsigned c0 = *(const GAS unsigned*)(cvec + ci), c1 = *(const GAS unsigned*)(cvec + D + ci);
  u32x4 v[6];
#pragma unroll
  for (int s = 0; s < 3; ++s) v[s] = __builtin_nontemporal_load((const GAS u32x4*)(base0 + s * st0));
  if (is_mod) {
#pragma unroll
    for (int s = 3; s < 6; ++s) v[s] = __builtin_nontemporal_load((const GAS u32x4*)(base0 + s * st0));
  }
  if (is_ln) {
    float* sm = sm2[0];
    STAMP(11);
    float sum = 0.f;
#pragma unroll
    for (int s = 0; s < 3; ++s)
#pragma unroll
      for (int j = 0; j < 4; ++j) sum += bf_lo(v[s][j]) + bf_hi(v[s][j]);
    const float mw = xhalf_sum(x16_sum(sum)) * (1.f / 96.f);
    STAMP(12);
    float sq = 0.f;
#pragma unroll
    for (int s = 0; s < 3; ++s)
#pragma unroll
      for (int j = 0; j < 4; ++j) { const float a = bf_lo(v[s][j]) - mw, c = bf_hi(v[s][j]) - mw; sq = fmaf(a, a, sq); sq = fmaf(c, c, sq); }
    sq = xhalf_sum(x16_sum(sq));
    if (lane < 16) *(f32x2*)(sm + (r * 8 + w) * 2) = f32x2{mw, sq};
    __syncthreads();
    f32x4 p[4];
#pragma unroll
    for (int q = 0; q < 4; ++q) p[q] = *(const f32x4*)(sm + r * 16 + q * 4);
    const float mean = ((p[0][0] + p[0][2]) + (p[1][0] + p[1][2]) + (p[2][0] + p[2][2]) + (p[3][0] + p[3][2])) * 0.125f;
    float m2 = 0.f, dd = 0.f;
#pragma unroll
    for (int q = 0; q < 4; ++q) { const float d0 = p[q][0] - mean, d1 = p[q][2] - mean; m2 += p[q][1] + p[q][3]; dd = fmaf(d0, d0, fmaf(d1, d1, dd)); }
    const float rstd = 1.f / sqrtf((m2 + 96.f * dd) * (1.f / D) + 1e-5f);
    char* dst = ws + WS_XN + (size_t)tt * TILE_B + (size_t)(6 * w + kk) * 1024 + (h * 32 + half * 16 + r) * 16;
#pragma unroll
    for (int s = 0; s < 3; ++s) {
      u32x4 o;
#pragma unroll
      for (int j = 0; j < 4; ++j) o[j] = pk_bf16((bf_lo(v[s][j]) - mean) * rstd, (bf_hi(v[s][j]) - mean) * rstd);
      gst16(dst + s * 2048, o);
    }
    STAMP(13); WAIT_VM(0); STAMP(14); STAMP_OUT(0);
  } else if (is_mod) {
    mod_slab<256, D / MSPLIT, false, true>(wmod_img, cvec, ksp * (D / MSPLIT), slab * 64,
                              (float*)(ws + WS_MOD) + (size_t)(type * MSPLIT + ksp) * 2 * 2304 + slab * 64, sm2[threadIdx.x >> 8], threadIdx.x & 255, v, c0, c1);
    STAMP(5); WAIT_VM(0); STAMP(6); STAMP_OUT(0);
  } else {
    const int pw = bid - (K0_LNWG + K0_MODWG / 2);
    if (pw == 0 && threadIdx.x < F_N) *(unsigned*)(ws + WS_FLAG + threadIdx.x * 256) = 0u;
    if (pw == 1) {
      unsigned lo, hi;
      asm volatile("s_getpc_b64 s[20:21]\n\ts_add_u32 %0, s20, _Z6k_mainILi7EEv4Args@rel32@lo+4\n\ts_addc_u32 %1, s21, _Z6k_mainILi7EEv4Args@rel32@hi+12"
                   : "=s"(lo), "=s"(hi)::"s20", "s21", "scc");
      const char* code = (const char*)(((unsigned long long)hi << 32) | lo) + threadIdx.x * 16;
      u32x4 c[CODE_KB / 8];
#pragma unroll
      for (int i = 0; i < CODE_KB / 8; ++i) c[i] = *(const GAS u32x4*)(code + i * 8192);
#pragma unroll
      for (int i = 0; i < CODE_KB / 8; ++i) asm volatile("" ::"v"(c[i]));
    }
    asm volatile("" ::"v"(v[0]), "v"(v[1]), "v"(v[2]));
  }
  RT_OUT(0);
}

#define BAR() __builtin_amdgcn_s_barrier()
#define WAIT_LG(n) asm volatile("s_waitcnt lgkmcnt(%0)" ::"n"(n) : "memory")
constexpr int imin(int a, int b) { return a < b ? a : b; }
constexpr int imax(int a, int b) { return a > b ? a : b; }
constexpr int NSTG = 24;
constexpr int LDS_MAIN = 163840;
constexpr int ld_cnt(int k, int ns, int n) { return k == 0 ? 3 : imin(imin(imin(n, k + 12), ld_cnt(k - 1, ns, n) + 2), imax(ns, k - 1 + ns - 1)); }
DI void bars(int n) {
#pragma unroll 1
  for (int i = 0; i < n; ++i) BAR();
}
template <int N, int NS, int PER, bool PRE, class F> DI void ring_loader(F&& issue) {
  sfor<0, N>([&](auto K) {
    constexpr int k = decltype(K)::v;
    sfor<(k == 0 ? 0 : ld_cnt(k - 1, NS, N)), ld_cnt(k, NS, N)>(issue);
    if constexpr (PRE && k == 0) BAR();
    WAIT_VM(PER * (ld_cnt(k, NS, N) - k - 1)); BAR();
  });
}
constexpr int ld2_cnt(int k, int ns, int n) { return k == 0 ? 6 : imin(imin(n, ld2_cnt(k - 1, ns, n) + 4), imax(ns, k - 1 + ns - 1)); }
template <int N, int NS, int PER, int PAR, class F> DI void ring_loader2(F&& issue, bool pre) {
  sfor<0, N>([&](auto K) {
    constexpr int k = decltype(K)::v, c = ld2_cnt(k, NS, N);
    sfor<(k == 0 ? 0 : ld2_cnt(k - 1, NS, N)), c>([&](auto G) { if constexpr ((decltype(G)::v & 1) == PAR) issue(G); });
    if constexpr (k == 0) { if (pre) BAR(); }
    WAIT_VM(PER * ((c + 1 - PAR) / 2 - (k + 2 - PAR) / 2)); BAR();
  });
}
DI void glds2x2(unsigned v, const char* sb0, const char* sb1m  , unsigned lds) {
  asm volatile(
      "s_mov_b32 m0, %3\n\ts_nop 0\n\t"
      "global_load_lds_dwordx4 %0, %1 sc1\n\t"
      "global_load_lds_dwordx4 %0, %1 offset:1024 sc1\n\t"
      "global_load_lds_dwordx4 %0, %2 offset:2048 sc1\n\t"
      "global_load_lds_dwordx4 %0, %2 offset:3072 sc1"
      ::"v"(v), "s"(sb0), "s"(sb1m), "s"(lds) : "memory");
}
DI void flag_add(char* ws, int f) { __hip_atomic_fetch_add((GAS unsigned*)(ws + WS_FLAG + f * 256), 1u, __ATOMIC_RELAXED, __HIP_MEMORY_SCOPE_AGENT); }
template <bool FUSED, int NAP = 3> DI void flag_wait(char* ws, int f  , unsigned want  ) {
  if constexpr (FUSED) {
    GAS unsigned* p = (GAS unsigned*)(ws + WS_FLAG + f * 256);
#pragma unroll 1
    for (unsigned spins = 0; spins < (1u << 20); ++spins) {
      if (__all(__hip_atomic_load(p, __ATOMIC_RELAXED, __HIP_MEMORY_SCOPE_AGENT) >= want)) break;
      __builtin_amdgcn_s_sleep(NAP);
    }
    asm volatile("" ::: "memory");
  }
}

DI void w_voff(unsigned (&voff)[5], int lane, unsigned pitch) {
#pragma unroll
  for (int i = 0; i < 5; ++i) {
    const unsigned o = i * 64 + lane, row = o / 20, cb = (o % 20) * 16;
    voff[i] = row * pitch + cb - (i < 4 ? i * 1024 : 0);
  }
}

struct Args {
  const char *x_img, *x_txt, *wqkv_img, *wqkv_txt, *wo_img, *wo_txt;
  const unsigned short *cvec, *wmod_img, *wmod_txt;
  char *ws, *out;
  unsigned task[256];
};

#define HD0 1
constexpr int W_SLOT = 10240, P1_NW = 10, P1_NH = 6, HD = 4, PF_START = 10;
constexpr int P1_H = P1_NW * W_SLOT;
constexpr int P1_MV = P1_H + P1_NH * 8192;
DI void phase1(const Args& A, const char* A_code, char* smem, int wave, int lane STAMP_ARG) {
  char* ws = A.ws;
  const int bid = blockIdx.x, xcd = bid & 7, idx = bid >> 3;
  const int rowhalf = xcd & 1, colq = xcd >> 1, h = lane >> 5;
  const bool txt = (colq == 3) & (idx >= 24);
  const int ct = sel(txt, rowhalf * 8 + (idx - 24), colq * 4 + (idx >> 3));
  const int n0 = sel(ct < 14, ct * 160, 2144);
  const unsigned lds0 = (unsigned)(size_t)(LAS char*)smem;
  const unsigned l16 = lane * 16;
  const int j = wave & 3;
  const char* xb = ws + WS_XN + (unsigned)(sel(txt, 32, rowhalf * TPB + (idx & 7) * 4) * TILE_B + j * 1024);
  const char* W = sel(txt, A.wqkv_txt, A.wqkv_img) + n0 * 2;
  const unsigned mr = imin(threadIdx.x, 383);
  const char* mp = ws + WS_MOD + (unsigned)(sel(txt, MSPLIT * 2, rowhalf) * 2304 * 4);
  asm volatile("s_nop 4" ::"s"(xb), "s"(W), "s"(mp) : "memory");
  if (ct >= 15) return;
  u32x4 m[MSPLIT];
  auto ldm = [&](const char* q) {
    const unsigned src = (mr < 192 ? 192 + mr : mr - 192) * 16;
    sfor<0, MSPLIT>([&](auto K) { constexpr int k = decltype(K)::v; gld16<0>(m[k], src, q + k * 18432); });
  };
  auto stm1 = [&](int bb) {
    tie(m[0], m[1], m[2], m[3]);
    auto f = [](u32x4 v) { return __builtin_bit_cast(f32x4, v); };
    *(f32x4*)(smem + P1_MV + bb * 6144 + mr * 16) = (f(m[0]) + f(m[1])) + (f(m[2]) + f(m[3])) + (mr < 192 ? 1.f : 0.f);
  };
  auto stm = [&]() {
    if (wave < 6) {
      stm1(0);
      if (txt) { ldm(mp + 2304 * 4); WAIT_VM(0); stm1(1); }
    }
    WAIT_LG(0);
    BAR();
  };
  if (__builtin_expect(wave >= 8, 1)) {
    if (wave >= 10) {
      u32x4 junk = {0, 0, 0, 0};
      const char* wo = sel((bool)(bid & 1), A.wo_txt, A.wo_img) + (bid >> 1) * 9216;
      const char* wg = (const char*)sel((bool)(bid & 1), A.wmod_txt, A.wmod_img) + (unsigned)((bid >> 1) * 6 * 9216 + 3072);
      const char* src = uni(sel(wave == 10, wo, wg));
      if (wave == 11 && idx < CODE_KB / 2) {
        const char* code = A_code + idx * 2048;
        touch16<0>(junk, l16, code); touch16<1024>(junk, l16, code);
      }
      bars(PF_START);
#pragma unroll 1
      for (int i = 0; i < 9; ++i) {
        const unsigned o = i * 64 + lane, row = o / 96;
        touch16<0>(junk, sel(wave == 10, i * 1024 + l16, row * 9216 + (o - row * 96) * 16), src);
        BAR();
      }
      bars(NSTG + 1 - 9 - PF_START);
      WAIT_VM(0); tie(junk);
    } else {
      unsigned voff[5];
      w_voff(voff, lane, 4608);
      const char* p = W + (wave - 8) * 16 * 4608;
      const unsigned lds = lds0 + (wave - 8) * 5120;
      ring_loader<NSTG, P1_NW, 5, true>([&](auto G) { glds5w(voff, p, lds + (decltype(G)::v % P1_NW) * W_SLOT); p += 32 * 4608; });
    }
  } else if (wave >= 4) {
    const unsigned x1 = l16 + sel(txt, TPB, 1) * TILE_B, x2 = l16 + sel(txt, 0, 2) * TILE_B, x3 = l16 + sel(txt, TPB, 3) * TILE_B;
    u32x4 xr[12][4];
    auto ldx = [&](auto I, auto U) {
      constexpr int i = decltype(I)::v, u = decltype(U)::v;
      if constexpr (i < 12) { gld16<0>(xr[i][u], u ? x1 : l16, xb + i * 4096); gld16<0>(xr[i][u + 2], u ? x3 : x2, xb + i * 4096); }
    };
    if (wave < 6) ldm(mp);
    sfor<0, HD0>([&](auto I) { ldx(I, IC<0>{}); ldx(I, IC<1>{}); });
    WAIT_VM(4 * HD0);
    stm();
    ldx(IC<1>{}, IC<0>{}); ldx(IC<1>{}, IC<1>{});
    const unsigned mva0 = lds0 + P1_MV + h * 32 + j * 64, mva1 = mva0 + sel(txt, 6144, 0);
    char* hw = smem + P1_H + ((j >> 1) * 2 + (j & 1)) * 4096 + l16;
    f32x4 mv[2][4];
    auto rdm = [&](auto I, auto U) {
      constexpr int i = decltype(I)::v, u = decltype(U)::v;
      const unsigned a = u ? mva1 : mva0;
      if constexpr (i < 12) { lds_128f<i * 256>(mv[u][0], a); lds_128f<i * 256 + 16>(mv[u][1], a); lds_128f<3072 + i * 256>(mv[u][2], a); lds_128f<3072 + i * 256 + 16>(mv[u][3], a); }
    };
    auto modw = [&](auto I, auto T) {
      constexpr int i = decltype(I)::v, t = decltype(T)::v, q = t & 1;
      const u32x4 x = xr[i][t];
      u32x4 o;
#pragma unroll
      for (int e = 0; e < 4; ++e)
        o[e] = mod_pair(x[e], mv[q][e >> 1][(e & 1) * 2], mv[q][2 + (e >> 1)][(e & 1) * 2], mv[q][e >> 1][(e & 1) * 2 + 1], mv[q][2 + (e >> 1)][(e & 1) * 2 + 1]);
      *(u32x4*)(hw + ((2 * i) % P1_NH) * 8192 + t * 1024) = o;
    };
    rdm(IC<0>{}, IC<0>{}); rdm(IC<0>{}, IC<1>{});
    sfor<0, 12>([&](auto I) {
      constexpr int i = decltype(I)::v;
      constexpr int ahead = imin(HD - 1, 11 - i), mvn = i < 11 ? 4 : 0;
      WAIT_VM(i ? 4 * ahead + 2 : 6); WAIT_LG(i ? 6 : 4);
      modw(I, IC<0>{}); modw(I, IC<2>{});
      if constexpr (i > 0) ldx(IC<i + HD>{}, IC<0>{});
      rdm(IC<i + 1>{}, IC<0>{});
      if constexpr (i > 0) { WAIT_LG(2 + mvn); if (i > 1 || j >= 2) BAR(); }
      else WAIT_LG(2 + mvn);
      WAIT_VM(i ? 4 * ahead + (i + HD < 12 ? 2 : 0) : 4);
      modw(I, IC<1>{}); modw(I, IC<3>{});
      if constexpr (i > 0) ldx(IC<i + HD>{}, IC<1>{});
      if constexpr (i == 0) WAIT_LG(0);
      rdm(IC<i + 1>{}, IC<1>{});
      WAIT_LG(2 + mvn);
      BAR();
      if constexpr (i == 0) sfor<2, HD + 1>([&](auto G) { ldx(G, IC<0>{}); ldx(G, IC<1>{}); });
    });
    WAIT_LG(0);
    BAR();
    if (j < 2) BAR();
  } else {
    ldm(mp); WAIT_VM(0); stm();
    STAMP(2);
    f32x16 acc[5];
    const int blk = (lane >> 4) & 1, q4 = (lane & 15) >> 2, p = lane & 3;
    unsigned rw = lds0 + (8 * h + q4) * 320 + blk * 32 + p * 8, rw1 = rw + 5 * W_SLOT;
    unsigned rh = lds0 + P1_H + wave * 1024 + l16;
    tie(rw, rw1, rh);
    u32x4 wf[2][5], hh[2];
    auto rdw = [&](auto S, auto C) {
      constexpr int s = decltype(S)::v, c = decltype(C)::v, cur = s & 1, slot = (s >> 1) % P1_NW, so = (slot % 5) * W_SLOT + (s & 1) * 5120;
      if constexpr (s < KS) {
        if constexpr (c == 0) lds_128<((s >> 1) % P1_NH) * 8192 + (s & 1) * 4096>(hh[cur], rh);
        lds_tr2<so + c * 64, so + c * 64 + 1280>(wf[cur][c], slot < 5 ? rw : rw1);
      }
    };
    STAMP(3);
    BAR();
    STAMP(4);
    sfor<0, 5>([&](auto C) { rdw(IC<0>{}, C); });
    sfor<0, KS>([&](auto S) {
      constexpr int s = decltype(S)::v, cur = s & 1;
      constexpr bool more = s + 1 < KS;
      if constexpr ((s & 1) && more) BAR();
      sfor<0, 5>([&](auto C) {
        constexpr int c = decltype(C)::v;
        WAIT_LG((4 - c) * 2 + (more ? 2 * c + (c > 0) : 0));
        mfma_v<s == 0>(acc[c], wf[cur][c], hh[cur]);
        rdw(IC<s + 1>{}, C);
      });
    });
    asm volatile("s_nop 15" : "+v"(acc[0]), "+v"(acc[1]), "+v"(acc[2]), "+v"(acc[3]), "+v"(acc[4]));
    STAMP(7);
    const int tt = sel(txt, (wave & 1) * TPB + 32, rowhalf * TPB + (idx & 7) * 4 + wave);
    if (!(txt && wave >= 2)) {
#pragma unroll
      for (int c = 0; c < 5; ++c) {
        const int f0 = n0 + c * 32;
        if (ct == 14 && c < 3) continue;
        const int mat = f0 / D, st = (f0 % D) >> 4;
        char* dst = ws + WS_QKV + ((size_t)mat * NTILE + tt) * TILE_B + st * 1024 + l16;
        gst16(dst, pack8(acc[c], 0));
        gst16(dst + 1024, pack8(acc[c], 1));
      }
    }
    WAIT_VM(0);
    STAMP(8);
  }
  BAR();
  if (threadIdx.x == 0) {
    const int rb = sel(txt, 16, rowhalf * 8 + (idx & 7)), m0 = (ct * 160) / D, m1 = imin(ct * 160 + 159, 2303) / D;
    flag_add(ws, F_QKV + rb * 3 + m0);
    if (m1 != m0) flag_add(ws, F_QKV + rb * 3 + m1);
  }
}

constexpr int P2_NS = 14, P2_RA = 10;
template <int MODE>
DI void p2_consumer(f32x16 (&acc)[5], const char* priv, unsigned lds0, int lane STAMP_ARG) {
  const int h = lane >> 5, blk = (lane >> 4) & 1, q4 = (lane & 15) >> 2, p = lane & 3;
  const unsigned l16 = lane * 16;
  constexpr int PW = MODE == 2 ? 1 : 2;
  u32x4 xr[KS], wf[2][5];
  auto ldx = [&](auto S) { constexpr int s = decltype(S)::v; if constexpr (s < KS) gld16_sc1<(s & 3) * 1024>(xr[s], l16, priv + (s >> 2) * 4096); };
  sfor<0, P2_RA>(ldx);
  unsigned rw = MODE == 1 ? lds0 + (4 * h + q4) * 320 + blk * 32 + p * 8 : lds0 + l16, rw1 = rw + 5 * W_SLOT, rw2 = rw + 10 * W_SLOT;
  tie(rw, rw1, rw2);
  auto rdw = [&](auto S, auto C) {
    constexpr int s = decltype(S)::v, c = decltype(C)::v, cur = s & 1, slot = (s >> 1) % P2_NS, so = (slot % 5) * W_SLOT + (s & 1) * 5120;
    if constexpr (s < KS) {
      const unsigned a = slot < 5 ? rw : (slot < 10 ? rw1 : rw2);
      if constexpr (MODE == 2) lds_128<so + c * 1024>(wf[cur][c], a);
      else lds_tr2<so + c * 64, so + c * 64 + 2560>(wf[cur][c], a);
    }
  };
  STAMP(11);
  BAR();
  STAMP(12);
  sfor<0, 5>([&](auto C) { rdw(IC<0>{}, C); });
  sfor<0, KS>([&](auto S) {
    constexpr int s = decltype(S)::v, cur = s & 1;
    constexpr bool more = s + 1 < KS;
    if constexpr ((s & 1) && more) BAR();
    WAIT_VM(imin(P2_RA - 1, KS - 1 - s));
    sfor<0, 5>([&](auto C) {
      constexpr int c = decltype(C)::v;
      WAIT_LG((4 - c) * PW + (more ? c * PW : 0));
      if constexpr (MODE == 1) mfma_v<s == 0>(acc[c], xr[s], wf[cur][c]);
      else mfma_v<s == 0>(acc[c], wf[cur][c], xr[s]);
      rdw(IC<s + 1>{}, C);
      if constexpr (c == 2) ldx(IC<s + P2_RA>{});
    });
  });
  asm volatile("s_nop 15" : "+v"(acc[0]), "+v"(acc[1]), "+v"(acc[2]), "+v"(acc[3]), "+v"(acc[4]));
  STAMP(15);
}
template <int NT> DI void p2_softmax(f32x16 (&acc)[5], char* dst  , float* st, int own0, int lane) {
  float m = -3.0e38f;
#pragma unroll
  for (int c = 0; c < NT; ++c)
    if (c >= own0) {
#pragma unroll
      for (int r = 0; r < 16; ++r) m = fmaxf(m, acc[c][r]);
    }
  m = xhalf_max(m);
  const float mc = -m * SM_C;
  float l = 0.f;
#pragma unroll
  for (int c = 0; c < NT; ++c)
    if (c >= own0) {
#pragma unroll
      for (int r = 0; r < 16; ++r) { const float pv = __builtin_amdgcn_exp2f(fmaf(acc[c][r], SM_C, mc)); acc[c][r] = pv; l += pv; }
      gst16(dst + c * 2048, pack8(acc[c], 0));
      gst16(dst + c * 2048 + 1024, pack8(acc[c], 1));
    }
  l = xhalf_sum(l);
  __hip_atomic_store((GAS float*)st + lane, lane < 32 ? m : l, __ATOMIC_RELAXED, __HIP_MEMORY_SCOPE_AGENT);
}
template <bool FUSED> DI void phase2(const Args& A, unsigned task, char* smem, int wave, int lane STAMP_ARG) {
  char* ws = A.ws;
  const int b = (blockIdx.x >> 2) & 1, kind = task & 7;
  const int pb = (task >> 3) & 15;
  const int sc = (task >> 7) & 15;
  const unsigned lds0 = (unsigned)(size_t)(LAS char*)smem;
  const unsigned l16 = lane * 16;
  const bool isS = kind == 1;
  const int kt0 = sel(sc < 6, sc * 5, 28);
  const int n0 = sel(sc < 4, sc * 160, 608);
  if (kind == 0) return;
  if (kind == 4) {
    u32x4 w[8];
    mod_slab<768, D, true, false>(pb ? A.wmod_txt : A.wmod_img, A.cvec, 0, 1536 + sc * 64, (float*)(ws + WS_MOD) + (size_t)pb * MSPLIT * 2 * 2304 + 1536 + sc * 64, (float*)smem, threadIdx.x, w, 0u, 0u);
    WAIT_VM(0);
    BAR();
    STAMP(17);
    if (threadIdx.x == 0) flag_add(ws, F_GATE + pb * 12 + sc);
    return;
  }
  if (kind == 3) {
    const int qt = b * TPB + 32, own0 = sc < 6 ? 0 : 2;
    float* xm = (float*)(smem + KS * 1024);
    if (wave == 5) flag_wait<FUSED>(ws, sel(lane & 2, F_QKV + 16 * 3, F_QKV + 1 + 3 * sel(lane & 1, sc < 6 ? b * 8 + ((kt0 + 4) >> 2) : 16, b * 8 + (kt0 >> 2))), sel(lane & 2, 5u, 6u));
    BAR();
    if (wave >= 8) {
      const char* q = uni(ws + WS_QKV + (unsigned)(qt * TILE_B + (wave - 8) * 12288));
      asm volatile("s_nop 4" ::"s"(q) : "memory");
      sfor<0, 3>([&](auto I) { constexpr int i = decltype(I)::v; gldsN<4>(l16, q + i * 4096, lds0 + (wave - 8) * 12288 + i * 4096); });
      WAIT_VM(0);
      bars(3);
    } else if (wave >= 5) bars(3);
    else {
      const int kt = kt0 + wave;
      const char* kp = uni(ws + WS_QKV + (unsigned)((NTILE + b * TPB + kt) * TILE_B));
      asm volatile("s_nop 4" ::"s"(kp) : "memory");
      constexpr int RA = 24;
      u32x4 kf[KS], qf[KS];
      auto ldk = [&](auto S) { constexpr int s = decltype(S)::v; if constexpr (s < KS) gld16_sc1<(s & 3) * 1024>(kf[s], l16, kp + (s >> 2) * 4096); };
      auto rdq = [&](auto S) { constexpr int s = decltype(S)::v; if constexpr (s < KS) lds_128<s * 1024>(qf[s], lds0 + l16); };
      sfor<0, RA>(ldk);
      BAR();
      sfor<0, 4>(rdq);
      f32x16 acc;
      sfor<0, KS>([&](auto S) {
        constexpr int s = decltype(S)::v;
        WAIT_VM(imin(RA - 1, KS - 1 - s)); WAIT_LG(imin(3, KS - 1 - s));
        mfma_v<s == 0>(acc, kf[s], qf[s]);
        rdq(IC<s + 4>{}); ldk(IC<s + RA>{});
      });
      asm volatile("s_nop 15" : "+v"(acc));
      float m = acc[0];
#pragma unroll
      for (int r = 1; r < 16; ++r) m = fmaxf(m, acc[r]);
      m = xhalf_max(m);
      xm[wave * 32 + (lane & 31)] = wave >= own0 ? m : -3.0e38f;
      WAIT_LG(0);
      BAR();
#pragma unroll
      for (int w = 0; w < 5; ++w) m = w ? fmaxf(m, xm[w * 32 + (lane & 31)]) : xm[lane & 31];
      const float mc = -m * SM_C;
      float l = 0.f;
#pragma unroll
      for (int r = 0; r < 16; ++r) { const float pv = __builtin_amdgcn_exp2f(fmaf(acc[r], SM_C, mc)); acc[r] = pv; l += pv; }
      if (wave >= own0) {
        char* dst = ws + WS_P + (size_t)qt * PT_B + (size_t)kt * 2048 + l16;
        gst16(dst, pack8(acc, 0)); gst16(dst + 1024, pack8(acc, 1));
      }
      l = xhalf_sum(l);
      xm[160 + wave * 32 + (lane & 31)] = wave >= own0 ? l : 0.f;
      WAIT_LG(0);
      BAR();
      if (wave == 0) {
        l = 0.f;
#pragma unroll
        for (int w = 0; w < 5; ++w) l += xm[160 + w * 32 + (lane & 31)];
        __hip_atomic_store((GAS float*)(ws + WS_ST) + ((size_t)qt * NCH + sc) * 64 + lane, lane < 32 ? m : l, __ATOMIC_RELAXED, __HIP_MEMORY_SCOPE_AGENT);
      }
      WAIT_VM(0);
    }
    BAR();
    STAMP(16);
    if (threadIdx.x == 0) flag_add(ws, F_P + b * 9 + 8);
    return;
  }
  if (__builtin_expect(wave >= 4, 1)) {
    if (wave >= 8) {
      if (wave == 8) flag_wait<FUSED>(ws, sel((lane & 2) || !isS, F_QKV + sel(pb < 8, b * 8 + pb, 16) * 3 + sel(isS, 0, 2),
                                              F_QKV + 1 + 3 * sel(lane & 1, sc < 6 ? b * 8 + ((kt0 + 4) >> 2) : 16, b * 8 + (kt0 >> 2))), sel((lane & 2) && isS, 5u, 6u));
      bars(NSTG + 1);
    } else {
      unsigned voff[5];
      w_voff(voff, lane, 1536);
      if (isS) {
#pragma unroll
        for (int i = 0; i < 5; ++i) voff[i] = i * TILE_B + l16 - (i < 4 ? i * 1024 : 0);
      }
      const unsigned stride = __builtin_amdgcn_readfirstlane(sel(isS, 2048, 32 * 1536)), lds = lds0 + (wave & 1) * 5120;
      const char* p = uni(sel(isS, (const char*)ws + WS_QKV + (unsigned)((NTILE + b * TPB + kt0) * TILE_B + (wave & 1) * 1024), A.wo_img + (n0 * 2 + (wave & 1) * 16 * 1536)));
      p = sadd(p, ((wave >> 1) & 1) * stride);
      asm volatile("s_nop 4" ::"s"(p), "s"(stride) : "memory");
      auto issue = [&](auto G) { glds5(voff, p, lds + (decltype(G)::v % P2_NS) * W_SLOT); p = sadd(p, 2 * stride); };
      if (isS) BAR();
      if (wave < 6) ring_loader2<NSTG, P2_NS, 5, 0>(issue, !isS); else ring_loader2<NSTG, P2_NS, 5, 1>(issue, !isS);
    }
  } else {
    f32x16 acc[5];
    const int tl = sel(pb < 8, pb * 4 + wave, 32);
    const bool active = pb < 8 || wave == 0;
    const char* priv = uni(ws + WS_QKV + (unsigned)((sel(isS, 0, 2 * NTILE) + b * TPB + tl) * TILE_B));
    asm volatile("s_nop 4" ::"s"(priv) : "memory");
    BAR();
    if (isS) {
      const int qt = b * TPB + tl;
      p2_consumer<2>(acc, priv, lds0, lane STAMP_PASS);
      p2_softmax<5>(acc, ws + WS_P + (size_t)qt * PT_B + (size_t)kt0 * 2048 + l16, (float*)(ws + WS_ST) + ((size_t)qt * NCH + sc) * 64, sc < 6 ? 0 : 2, lane);
    } else {
      p2_consumer<1>(acc, priv, lds0, lane STAMP_PASS);
      if (active) {
        char* dst = ws + WS_VP + ((size_t)b * 24 + (n0 >> 5)) * PT_B + (size_t)tl * 2048 + l16;
#pragma unroll
        for (int c = 0; c < 5; ++c) {
          if (sc == 4 && c == 0) continue;
          gst16(dst + (size_t)c * PT_B, pack8(acc[c], 0));
          gst16(dst + (size_t)c * PT_B + 1024, pack8(acc[c], 1));
        }
      }
    }
    STAMP(16);
    WAIT_VM(0);
    STAMP(17);
  }
  BAR();
  if (threadIdx.x == 0) flag_add(ws, sel(isS, F_P + b * 9 + pb, F_VP + b * 5 + sc));
}

constexpr int P3_RA = 11;
constexpr int NSTG3 = 22;
constexpr int ld3_cnt(int k) { return k == 0 ? 2 : imin(NSTG3, k + 6); }
DI void fma4(f32x16& acc, const f32x16& t, float f, int e) {
  asm volatile("v_fmac_f32 %0, %4, %8\n\tv_fmac_f32 %1, %5, %8\n\tv_fmac_f32 %2, %6, %8\n\tv_fmac_f32 %3, %7, %8"
               : "+v"(acc[e]), "+v"(acc[e + 1]), "+v"(acc[e + 2]), "+v"(acc[e + 3]) : "v"(t[e]), "v"(t[e + 1]), "v"(t[e + 2]), "v"(t[e + 3]), "v"(f));
}
DI void chunk_scales(float (&f)[NCH], const float (&mm)[NCH], const float (&ll)[NCH]) {
  float m = mm[0];
#pragma unroll
  for (int c = 1; c < NCH; ++c) m = fmaxf(m, mm[c]);
  float l = 0.f;
#pragma unroll
  for (int c = 0; c < NCH; ++c) { f[c] = __builtin_amdgcn_exp2f((mm[c] - m) * SM_C); l = fmaf(f[c], ll[c], l); }
  const float inv = 1.f / l;
#pragma unroll
  for (int c = 0; c < NCH; ++c) f[c] *= inv;
}
DI void ld_stats(float (&mm)[NCH], float (&ll)[NCH], const char* stp, int r) {
  sfor<0, NCH>([&](auto C) { constexpr int c = decltype(C)::v; gld4<c * 256>(mm[c], r * 4, stp); gld4<c * 256 + 128>(ll[c], r * 4, stp); });
}
DI void tie_stats(float (&mm)[NCH], float (&ll)[NCH]) {
#pragma unroll
  for (int c = 0; c < NCH; ++c) tie(mm[c], ll[c]);
}
template <bool FUSED> DI void phase3(const Args& A, unsigned task, char* smem, int wave, int lane STAMP_ARG) {
  char* ws = A.ws;
  const int b = (blockIdx.x >> 2) & 1, kind = (task >> 11) & 3;
  const int r = lane & 31, h = lane >> 5;
  const unsigned lds0 = (unsigned)(size_t)(LAS char*)smem;
  const unsigned l16 = lane * 16;
  if (kind == 0) return;
  if (kind == 1) {
    const int qb = (task >> 13) & 7, dp = (task >> 16) & 31;
    if (wave >= 7) return;
    if (wave == 6) {
      flag_wait<FUSED>(ws, sel(lane & 2, sel(lane & 1, F_GATE + dp, F_P + b * 9 + qb), F_VP + b * 5 + imin((dp * 64 + (lane & 1) * 63) / 160, 4)),
                       sel(lane & 2, sel(lane & 1, 1u, 7u), 9u));
      BAR();
      return;
    }
    if (__builtin_expect(wave >= 4, 1)) {
      const char* p = uni(ws + WS_VP + (unsigned)((b * 24 + dp * 2 + (wave - 4)) * PT_B));
      const unsigned lds = lds0 + (wave - 4) * (KSK * 1024);
      asm volatile("s_nop 4" ::"s"(p) : "memory");
      BAR();
      sfor<0, NSTG3>([&](auto K) {
        constexpr int k = decltype(K)::v;
        sfor<(k == 0 ? 0 : ld3_cnt(k - 1)), ld3_cnt(k)>([&](auto G) { constexpr int g = decltype(G)::v; gldsN<3>(l16, p + g * 3072, lds + g * 3072); });
        WAIT_VM(3 * (ld3_cnt(k) - k - 1)); BAR();
      });
      return;
    }
    const int qt = b * TPB + qb * 4 + wave;
    const char* pb = uni(ws + WS_P + (unsigned)(qt * PT_B));
    const char* stp = uni(ws + WS_ST + (unsigned)(qt * NCH * 256));
    asm volatile("s_nop 4" ::"s"(pb), "s"(stp) : "memory");
    BAR();
    u32x4 pf[KSK];
    auto ldp = [&](auto S) { constexpr int s = decltype(S)::v; if constexpr (s < KSK) gld16_sc1<(s & 3) * 1024>(pf[s], l16, pb + (s >> 2) * 4096); };
    float mm[NCH], ll[NCH], f[NCH];
    sfor<0, 3>(ldp);
    ld_stats(mm, ll, stp, r);
    sfor<3, P3_RA>(ldp);
    f32x16 acc[2], tmp[2];
    acc[0] = f32x16{}; acc[1] = f32x16{};
    tie(acc[0], acc[1]);
    u32x4 vf[2][2];
    unsigned rv0 = lds0 + l16, rv1 = rv0 + KSK * 1024;
    tie(rv0, rv1);
    auto rdv = [&](auto S, auto T) {
      constexpr int s = decltype(S)::v, t = decltype(T)::v;
      if constexpr (s < 64) lds_128<s * 1024>(vf[s & 1][t], t ? rv1 : rv0);
      else if constexpr (s < KSK) lds_128<(s - 2) * 1024>(vf[s & 1][t], (t ? rv1 : rv0) + 2048);
    };
    u32x4 xv[4];
    float gl;
    const unsigned orow = ((b * NI + (qb * 4 + wave) * 32 + (lane >> 3)) * D + dp * 64) * 2 + (lane & 7) * 16;
    STAMP(19);
    BAR();
    STAMP(20);
    rdv(IC<0>{}, IC<0>{}); rdv(IC<0>{}, IC<1>{}); rdv(IC<1>{}, IC<0>{}); rdv(IC<1>{}, IC<1>{});
    sfor<0, KSK>([&](auto S) {
      constexpr int s = decltype(S)::v, cur = s & 1, c = s / 10, cs = s % 10;
      if constexpr ((s + 2) % 3 == 0 && s + 2 < KSK) BAR();
      WAIT_VM(imin(P3_RA - 1, KSK - 1 - s) + (s < 3 ? 2 * NCH : 0) + (s > KSK - 1 - P3_RA ? 5 : 0));
      sfor<0, 2>([&](auto T) {
        constexpr int t = decltype(T)::v;
        WAIT_LG((1 - t) + (s + 1 < KSK ? 2 : 0) + (s + 2 < KSK ? t : 0));
        mfma_v<cs == 0>(tmp[t], vf[cur][t], pf[s]);
        rdv(IC<s + 2>{}, T);
        if constexpr (t == 0) {
          ldp(IC<s + P3_RA>{});
          if constexpr (s == KSK - 1 - P3_RA) {
            sfor<0, 4>([&](auto E) { constexpr int e = decltype(E)::v; gld16<0>(xv[e], orow, A.x_img + e * 8 * D * 2); });
            gld4<0>(gl, lane * 4, ws + WS_MOD + (unsigned)((b * 2304 + 1536 + dp * 64) * 4));
          }
        }
      });
      if constexpr (cs == 9) {
        asm volatile("s_nop 9" : "+v"(tmp[0]));
        fma4(acc[0], tmp[0], f[c], 0); fma4(acc[0], tmp[0], f[c], 4); fma4(acc[0], tmp[0], f[c], 8); fma4(acc[0], tmp[0], f[c], 12);
        fma4(acc[1], tmp[1], f[c], 0); fma4(acc[1], tmp[1], f[c], 4); fma4(acc[1], tmp[1], f[c], 8); fma4(acc[1], tmp[1], f[c], 12);
      }
      if constexpr (s == 4) { tie_stats(mm, ll); chunk_scales(f, mm, ll); SBAR(); }
    });
    asm volatile("s_nop 15" : "+v"(tmp[0]), "+v"(tmp[1]));
    WAIT_VM(0); tie(xv[0], xv[1], xv[2], xv[3], gl);
    BAR();
    STAMP(21);
    {
      char* tb = smem + wave * 8192;
      *(float*)(smem + 32768 + wave * 256 + lane * 4) = gl;
#pragma unroll
      for (int e = 0; e < 8; ++e) {
        const int t = e >> 2, gq = e & 3;
        f32x4 y;
#pragma unroll
        for (int k = 0; k < 4; ++k) y[k] = fmaf(tmp[t][4 * gq + k], f[6], acc[t][4 * gq + k]);
        *(f32x4*)(tb + r * 256 + (((2 * e + h) ^ (r & 15)) * 16)) = y;
      }
      const f32x4 g0 = *(const f32x4*)(smem + 32768 + wave * 256 + (lane & 7) * 32), g1 = *(const f32x4*)(smem + 32768 + wave * 256 + (lane & 7) * 32 + 16);
#pragma unroll
      for (int e = 0; e < 4; ++e) {
        const int row = e * 8 + (lane >> 3), c2 = (lane & 7) * 2;
        const f32x4 y0 = *(const f32x4*)(tb + row * 256 + ((c2 ^ (row & 15)) * 16));
        const f32x4 y1 = *(const f32x4*)(tb + row * 256 + (((c2 + 1) ^ (row & 15)) * 16));
        u32x4 o = {pk_bf16(fmaf(g0[0], y0[0], bf_lo(xv[e][0])), fmaf(g0[1], y0[1], bf_hi(xv[e][0]))), pk_bf16(fmaf(g0[2], y0[2], bf_lo(xv[e][1])), fmaf(g0[3], y0[3], bf_hi(xv[e][1]))),
                   pk_bf16(fmaf(g1[0], y1[0], bf_lo(xv[e][2])), fmaf(g1[1], y1[1], bf_hi(xv[e][2]))), pk_bf16(fmaf(g1[2], y1[2], bf_lo(xv[e][3])), fmaf(g1[3], y1[3], bf_hi(xv[e][3])))};
        gst16(A.out + orow + (size_t)e * 8 * D * 2, o);
      }
    }
    STAMP(22);
  } else {
    if (wave >= 8) return;
    const int T = (task >> 16) & 31;
    const int qt = b * TPB + 32;
    constexpr int LV = 0, LW = 8 * 10240, LR = LW + 768 * 64;
    if (wave == 7) {
      flag_wait<FUSED, 16>(ws, F_P + b * 9 + 8, 7u);
      flag_wait<FUSED>(ws, sel(lane < 9, F_QKV + 2 + 3 * sel(lane < 8, b * 8 + lane, 16), F_GATE + 12 + (T >> 1)), sel(lane < 9, 6u, 1u));
    }
    BAR();
    STAMP(1);
    const int g0 = sel(wave < 6, wave * 5, 28);
    const char* vsrc = uni(ws + WS_QKV + (unsigned)((2 * NTILE + b * TPB + g0) * TILE_B + T * 2048));
    const char* pA = uni(ws + WS_P + (unsigned)(qt * PT_B + g0 * 2048));
    const char* stp = uni(ws + WS_ST + (unsigned)(qt * NCH * 256));
    const char* wsrc = uni(A.wo_txt + (unsigned)(96 * wave * 1536 + T * 64));
    asm volatile("s_nop 4" ::"s"(vsrc), "s"(pA), "s"(stp), "s"(wsrc) : "memory");
    const unsigned lv = lds0 + LV + wave * 10240;
    sfor<0, 5>([&](auto K) { constexpr int kt = decltype(K)::v; gldsN<2>(l16, vsrc + kt * TILE_B, lv + kt * 2048); });
    u32x4 pa[10];
    sfor<0, 10>([&](auto S) { constexpr int s = decltype(S)::v; gld16_sc1<(s & 3) * 1024>(pa[s], l16, pA + (s >> 2) * 4096); });
    float mm[NCH], ll[NCH], f[NCH];
    ld_stats(mm, ll, stp, r);
    {
      const unsigned wv = (lane >> 2) * 1536 + (lane & 3) * 16;
      sfor<0, 6>([&](auto I) { constexpr int i = decltype(I)::v; gldsN<1>(wv, wsrc + i * 16 * 1536, lds0 + LW + (96 * wave + 16 * i) * 64); });
    }
    const int blk = (lane >> 4) & 1, q4 = (lane & 15) >> 2, p = lane & 3;
    const unsigned va = lv + blk * 1024 + ((p & 1) * 32 + 4 * h + q4) * 16 + (p >> 1) * 8;
    WAIT_VM(6);
    STAMP(10);
    tie_stats(mm, ll);
    tie(pa[0], pa[1], pa[2], pa[3], pa[4], pa[5], pa[6], pa[7], pa[8], pa[9]);
    f32x16 ta = f32x16{};
    {
      u32x4 v[10];
      auto rdv = [&](auto S) { constexpr int s = decltype(S)::v, o = (s >> 1) * 2048 + (s & 1) * 256; lds_tr2<o, o + 128>(v[s], va); };
      sfor<0, 7>(rdv);
      chunk_scales(f, mm, ll);
      const int skip = wave >= 6 ? 4 : 0;
      sfor<0, 10>([&](auto S) {
        constexpr int s = decltype(S)::v;
        WAIT_LG(2 * (imin(s + 7, 10) - s - 1)); tie(v[s]);
        if constexpr (s + 7 < 10) rdv(IC<s + 7>{});
        if (s >= skip) ta = mfma(v[s], pa[s], ta);
      });
    }
    float fa = f[0];
#pragma unroll
    for (int w = 1; w < 7; ++w) if (wave == w) fa = f[w];
    f32x4* red = (f32x4*)(smem + LR);
    if (wave < 7) {
#pragma unroll
      for (int e = 0; e < 4; ++e) red[(wave * 4 + e) * 64 + lane] = f32x4{ta[4 * e], ta[4 * e + 1], ta[4 * e + 2], ta[4 * e + 3]} * fa;
    }
    u32x2 xv; u32x4 gq;
    gld8<0>(xv, (r * D + 4 * h + (wave & 3) * 8) * 2, uni(A.x_txt + (unsigned)((b * NTX * D + T * 32) * 2)));
    gld16_sc1<0>(gq, h * 16 + (wave & 3) * 32, uni(ws + WS_MOD + (unsigned)(((MSPLIT * 2 + b) * 2304 + 1536 + T * 32) * 4)));
    WAIT_LG(0);
    BAR();
    if (wave < 2) {
      f32x4 s0 = red[(2 * wave) * 64 + lane], s1 = red[(2 * wave + 1) * 64 + lane];
#pragma unroll
      for (int w = 1; w < 7; ++w) { s0 += red[(w * 4 + 2 * wave) * 64 + lane]; s1 += red[(w * 4 + 2 * wave + 1) * 64 + lane]; }
      gst16(ws + WS_AT + (size_t)b * TILE_B + T * 2048 + wave * 1024 + l16, u32x4{pk_bf16(s0[0], s0[1]), pk_bf16(s0[2], s0[3]), pk_bf16(s1[0], s1[1]), pk_bf16(s1[2], s1[3])});
      WAIT_VM(0);
      STAMP(23);
      if (lane == 0) flag_add(ws, F_TXT + b);
    }
    u32x4 wq[6];
    WAIT_VM(0); tie(xv, gq);
    {
      const unsigned wa = lds0 + LW + (96 * wave + 4 * h + q4) * 64 + blk * 32 + p * 8;
      sfor<0, 6>([&](auto S) { constexpr int s = decltype(S)::v; lds_tr2<s * 1024, s * 1024 + 512>(wq[s], wa); });
    }
    if (wave == 7) flag_wait<true>(ws, F_TXT + b, 48u);
    WAIT_LG(0);
    BAR();
    u32x4 af[6];
    const char* ab = uni(ws + WS_AT + (unsigned)(b * TILE_B + wave * 6144));
    asm volatile("s_nop 4" ::"s"(ab) : "memory");
    sfor<0, 6>([&](auto S) { constexpr int s = decltype(S)::v; gld16_sc1<(s & 3) * 1024>(af[s], l16, ab + (s >> 2) * 4096); });
    WAIT_VM(0);
    tie(af[0], af[1], af[2], af[3], af[4], af[5], wq[0], wq[1], wq[2], wq[3], wq[4], wq[5]);
    f32x16 tb = f32x16{};
#pragma unroll
    for (int s = 0; s < 6; ++s) tb = mfma(wq[s], af[s], tb);
#pragma unroll
    for (int e = 0; e < 4; ++e) red[(wave * 4 + e) * 64 + lane] = f32x4{tb[4 * e], tb[4 * e + 1], tb[4 * e + 2], tb[4 * e + 3]};
    WAIT_LG(0);
    BAR();
    if (wave < 4) {
      f32x4 o = red[wave * 64 + lane];
#pragma unroll
      for (int w = 1; w < 8; ++w) o += red[(w * 4 + wave) * 64 + lane];
      const f32x4 gt = __builtin_bit_cast(f32x4, gq);
      u32x2 ov = {pk_bf16(fmaf(gt[0], o[0], bf_lo(xv[0])), fmaf(gt[1], o[1], bf_hi(xv[0]))), pk_bf16(fmaf(gt[2], o[2], bf_lo(xv[1])), fmaf(gt[3], o[3], bf_hi(xv[1])))};
      *(u32x2*)(A.out + ((size_t)2 * NI * D + ((size_t)b * NTX + r) * D + T * 32 + 4 * h + wave * 8) * 2) = ov;
    }
  }
}

template <int PH> __global__ __launch_bounds__(768, 3) void k_main(Args A) {
  extern __shared__ __attribute__((aligned(16))) char smem[];
  STAMP_DECL; STAMP(0); RT_IN(1);
  const int wave = __builtin_amdgcn_readfirstlane(threadIdx.x >> 6), lane = threadIdx.x & 63;
  const unsigned task = A.task[blockIdx.x];
  const char* code;
  asm volatile("s_getpc_b64 %0" : "=s"(code));
  if constexpr (PH & 1) phase1(A, code, smem, wave, lane STAMP_PASS);
  STAMP(9);
  RT_MID(0);
  if constexpr (PH & 2) phase2<PH == 7>(A, task, smem, wave, lane STAMP_PASS);
  STAMP(18);
  RT_MID(1);
  if constexpr (PH & 4) phase3<PH == 7>(A, task, smem, wave, lane STAMP_PASS);
}

#define ONE_LAUNCH 1
extern "C" void kernel_launch(void* const* d_in, const int* in_sizes, int n_in, void* d_out, int out_size, void* d_ws, size_t ws_size,
                              hipStream_t stream) {
  (void)in_sizes; (void)n_in; (void)out_size; (void)ws_size;
  Args A;
  A.x_img = (const char*)d_in[0]; A.x_txt = (const char*)d_in[1]; A.cvec = (const unsigned short*)d_in[2];
  A.wqkv_img = (const char*)d_in[3]; A.wqkv_txt = (const char*)d_in[4]; A.wo_img = (const char*)d_in[5]; A.wo_txt = (const char*)d_in[6];
  A.wmod_img = (const unsigned short*)d_in[7]; A.wmod_txt = (const unsigned short*)d_in[8];
  A.ws = (char*)d_ws; A.out = (char*)d_out;
  int gate = 0;
  for (int x = 0; x < 8; ++x) {
    const int sub = x & 3, qh = sub & 1, ch = sub >> 1;
    int img = 0, ntx = 0;
    for (int i = 0; i < 32; ++i) {
      unsigned t = 0; bool light = false;
      if (sub < 2) {
        if (i < 28) t = 1u | ((sub * 4 + (i & 3)) << 3) | ((i >> 2) << 7);
        else { light = true; const int sc = sub * 4 + i - 28; if (sc < 7) t = 3u | (8u << 3) | (sc << 7); }
      } else {
        const int v = (sub - 2) * 24 + i;
        if (i < 24 && v < 45) t = 2u | ((v / 5) << 3) | ((v % 5) << 7); else light = true;
      }
      if (light && t == 0 && gate < 12 * (x / 4 + 1)) { t = 4u | ((gate & 1) << 3) | ((gate >> 1) << 7); ++gate; }
      if (light && ntx < (sub < 2 ? 4 : 8)) { t |= (2u << 11) | ((sub < 2 ? sub * 4 + ntx : 8 + (sub - 2) * 8 + ntx) << 16); ++ntx; }
      else if (!light && img < 24) { t |= (1u << 11) | ((qh * 4 + (img & 3)) << 13) | ((ch * 6 + (img >> 2)) << 16); ++img; }
      else if (light && img < 24 && sub == 3) { t |= (1u << 11) | ((qh * 4 + (img & 3)) << 13) | ((ch * 6 + (img >> 2)) << 16); ++img; }
      A.task[i * 8 + x] = t;
    }
  }
  k0_prep<<<K0_GRID, 512, 0, stream>>>((const unsigned short*)A.x_img, (const unsigned short*)A.x_txt, A.cvec, A.wmod_img, A.wmod_txt, A.wqkv_img, A.wqkv_txt, A.ws);
  (void)hipFuncSetAttribute((const void*)k_main<7>, hipFuncAttributeMaxDynamicSharedMemorySize, LDS_MAIN);
  k_main<7><<<256, 768, LDS_MAIN, stream>>>(A);
}
```

```cpp
#include <hip/hip_runtime.h>
#include <hip/hip_bf16.h>
#include <stdint.h>

typedef short    bf16x8 __attribute__((ext_vector_type(8)));
typedef float    f32x16 __attribute__((ext_vector_type(16)));
typedef float    f32x4  __attribute__((ext_vector_type(4)));
typedef float    f32x2  __attribute__((ext_vector_type(2)));
typedef unsigned u32x4  __attribute__((ext_vector_type(4)));
typedef unsigned u32x2  __attribute__((ext_vector_type(2)));
typedef __bf16   bf16x2_t __attribute__((ext_vector_type(2)));

#define DI __device__ __forceinline__
#define LAS __attribute__((address_space(3)))
#define GAS __attribute__((address_space(1)))
#define SBAR() __builtin_amdgcn_sched_barrier(0)

constexpr int D = 768, NI = 1024, NTX = 32;
constexpr int TPB = 33;
constexpr int NTILE = 66;
constexpr int KS = 48;
#define PAD1 0
#define PAD2 0
constexpr int TILE_B = KS * 1024 + PAD1;
constexpr int KSK = 66;
constexpr int PT_B = KSK * 1024 + PAD2;
constexpr int NCH = 7;
constexpr int MSPLIT = 4;

constexpr size_t WS_FLAG = 0;
constexpr int F_QKV = 0, F_P = 51, F_VP = 69, F_GATE = 79, F_TXT = 103, F_N = 128;
constexpr size_t WS_MOD  = 32768;
constexpr size_t WS_XN   = WS_MOD + (size_t)2 * MSPLIT * 2 * 2304 * 4;
constexpr size_t WS_QKV  = WS_XN + (size_t)NTILE * TILE_B;
constexpr size_t WS_P    = WS_QKV + (size_t)3 * NTILE * TILE_B;
constexpr size_t WS_VP   = WS_P + (size_t)NTILE * PT_B;
constexpr size_t WS_ST   = WS_VP + (size_t)48 * PT_B;
constexpr size_t WS_AT   = WS_ST + (size_t)NTILE * NCH * 2 * 32 * 4;
constexpr size_t WS_END  = WS_AT + (size_t)2 * TILE_B;

constexpr float SM_C = 0.03608439182435161f * 1.4426950408889634f;

template <class T> DI T sel(bool c, T a, T b) { return c ? a : b; }
DI int imin0(int a, int b) { return a < b ? a : b; }
template <int I> struct IC { static constexpr int v = I; };
template <int B, int E, class F> DI void sfor(F&& f) { if constexpr (B < E) { f(IC<B>{}); sfor<B + 1, E>(f); } }

DI unsigned pk_bf16(float a, float b) { f32x2 v = {a, b}; bf16x2_t r = __builtin_convertvector(v, bf16x2_t); return __builtin_bit_cast(unsigned, r); }
DI float bf_lo(unsigned u) { return __uint_as_float(u << 16); }
DI float bf_hi(unsigned u) { return __uint_as_float(u & 0xffff0000u); }
DI f32x16 mfma(u32x4 a, u32x4 b, f32x16 c) {
  return __builtin_amdgcn_mfma_f32_32x32x16_bf16(__builtin_bit_cast(bf16x8, a), __builtin_bit_cast(bf16x8, b), c, 0, 0, 0);
}
template <bool FIRST> DI void mfma_v(f32x16& c, u32x4 a, u32x4 b) {
  if constexpr (FIRST) asm volatile("v_mfma_f32_32x32x16_bf16 %0, %1, %2, 0" : "=&v"(c) : "v"(a), "v"(b));
  else asm volatile("v_mfma_f32_32x32x16_bf16 %0, %1, %2, %0" : "+v"(c) : "v"(a), "v"(b));
}
DI float xhalf_max(float v) { auto r = __builtin_amdgcn_permlane32_swap(__float_as_uint(v), __float_as_uint(v), false, false); return fmaxf(__uint_as_float(r[0]), __uint_as_float(r[1])); }
DI float xhalf_sum(float v) { auto r = __builtin_amdgcn_permlane32_swap(__float_as_uint(v), __float_as_uint(v), false, false); return __uint_as_float(r[0]) + __uint_as_float(r[1]); }
DI u32x4 pack8(const f32x16& a, int s) {
  u32x4 r = {pk_bf16(a[8 * s], a[8 * s + 1]), pk_bf16(a[8 * s + 2], a[8 * s + 3]), pk_bf16(a[8 * s + 4], a[8 * s + 5]), pk_bf16(a[8 * s + 6], a[8 * s + 7])};
  return r;
}
DI unsigned xn_pack(float a, float b) {
  const unsigned lo = pk_bf16(a, 0.f);
  const unsigned m = __float_as_uint(fmaxf(fabsf(b), 1.17549435e-38f));
  const unsigned d = ((m + 0x8000u - lo) & 0x7fff0000u) | lo;
  return d | (__float_as_uint(b) & 0x80000000u);
}
DI unsigned mod_pair(unsigned x, float s0, float t0, float s1, float t1) {
  unsigned o, a, b;
  asm volatile("v_lshlrev_b32 %1, 16, %3\n\tv_fma_f32 %2, %3, %6, %7\n\tv_fma_f32 %1, %1, %4, %5\n\tv_cvt_pk_bf16_f32 %0, %1, %2"
      : "=v"(o), "=&v"(a), "=&v"(b) : "v"(x), "v"(s0), "v"(t0), "v"(s1), "v"(t1));
  return o;
}
DI const char* sadd(const char* p, unsigned d) {
  const unsigned long long u = (unsigned long long)p;
  unsigned lo, hi;
  asm("s_add_u32 %0, %2, %4\n\ts_addc_u32 %1, %3, 0" : "=&s"(lo), "=&s"(hi) : "s"((unsigned)u), "s"((unsigned)(u >> 32)), "s"(d) : "scc");
  return (const char*)(((unsigned long long)hi << 32) | lo);
}
DI const char* uni(const char* p) {
  unsigned long long u = (unsigned long long)p;
  unsigned lo = __builtin_amdgcn_readfirstlane((unsigned)u), hi = __builtin_amdgcn_readfirstlane((unsigned)(u >> 32));
  return (const char*)(((unsigned long long)hi << 32) | lo);
}

#define STAMP(i)
#define STAMP_DECL
#define STAMP_OUT(k)
#define STAMP_ARG
#define STAMP_PASS
#define RT_IN(k)
#define RT_OUT(k)
#define RT_MID(i)

#define ST_POL "sc1"
DI void gst16(char* p, u32x4 v) { asm volatile("global_store_dwordx4 %0, %1, off " ST_POL "\n\ts_nop 1" ::"v"(p), "v"(v) : "memory"); }
#define WAIT_VM(n) asm volatile("s_waitcnt vmcnt(%0)" ::"n"(n) : "memory")
#define WAIT_LG0() asm volatile("s_waitcnt lgkmcnt(0)" ::: "memory")
template <int OFF> DI void gld16(u32x4& d, unsigned voff, const char* sb) {
  asm volatile("global_load_dwordx4 %0, %1, %2 offset:%3" : "=v"(d) : "v"(voff), "s"(sb), "i"(OFF) : "memory");
}
template <int OFF> DI void gld16a(u32x4& d, unsigned voff, const char* sb) {
  asm volatile("global_load_dwordx4 %0, %1, %2 offset:%3" : "=a"(d) : "v"(voff), "s"(sb), "i"(OFF) : "memory");
}
template <int OFF> DI void gld8(u32x2& d, unsigned voff, const char* sb) {
  asm volatile("global_load_dwordx2 %0, %1, %2 offset:%3" : "=v"(d) : "v"(voff), "s"(sb), "i"(OFF) : "memory");
}
template <int OFF> DI void gld4(float& d, unsigned voff, const char* sb) {
  asm volatile("global_load_dword %0, %1, %2 offset:%3 sc1" : "=v"(d) : "v"(voff), "s"(sb), "i"(OFF) : "memory");
}
template <int OFF> DI void touch16(u32x4& junk, unsigned voff, const char* sb) {
  asm volatile("global_load_dwordx4 %0, %1, %2 offset:%3" : "+v"(junk) : "v"(voff), "s"(sb), "i"(OFF) : "memory");
}
DI void touch(unsigned& junk, unsigned voff, const char* sb) {
  asm volatile("global_load_dword %0, %1, %2" : "+v"(junk) : "v"(voff), "s"(sb) : "memory");
}
template <int OFF> DI void gld16_sc1(u32x4& d, unsigned voff, const char* sb) {
  asm volatile("global_load_dwordx4 %0, %1, %2 offset:%3 sc1" : "=v"(d) : "v"(voff), "s"(sb), "i"(OFF) : "memory");
}
#define GLDS5(NAME, POL) DI void NAME(const unsigned (&v)[5], const char* sb, unsigned lds) { \
  asm volatile("s_mov_b32 m0, %6\n\ts_nop 0\n\t" \
      "global_load_lds_dwordx4 %0, %5 " POL "\n\t" \
      "global_load_lds_dwordx4 %1, %5 offset:1024 " POL "\n\t" \
      "global_load_lds_dwordx4 %2, %5 offset:2048 " POL "\n\t" \
      "global_load_lds_dwordx4 %3, %5 offset:3072 " POL "\n\t" \
      "s_add_u32 m0, m0, 4096\n\ts_nop 0\n\t" \
      "global_load_lds_dwordx4 %4, %5 " POL \
      ::"v"(v[0]), "v"(v[1]), "v"(v[2]), "v"(v[3]), "v"(v[4]), "s"(sb), "s"(lds) : "memory", "scc"); }
#define W_POL ""
GLDS5(glds5, "sc1")
GLDS5(glds5w, W_POL)
template <int N> DI void gldsN(unsigned v, const char* sb, unsigned lds) {
  static_assert(N >= 1 && N <= 4, "");
  if constexpr (N == 1) asm volatile("s_mov_b32 m0, %2\n\ts_nop 0\n\tglobal_load_lds_dwordx4 %0, %1 sc1" ::"v"(v), "s"(sb), "s"(lds) : "memory");
  if constexpr (N == 2) asm volatile("s_mov_b32 m0, %2\n\ts_nop 0\n\tglobal_load_lds_dwordx4 %0, %1 sc1\n\tglobal_load_lds_dwordx4 %0, %1 offset:1024 sc1" ::"v"(v), "s"(sb), "s"(lds) : "memory");
  if constexpr (N == 3) asm volatile("s_mov_b32 m0, %2\n\ts_nop 0\n\tglobal_load_lds_dwordx4 %0, %1 sc1\n\tglobal_load_lds_dwordx4 %0, %1 offset:1024 sc1\n\tglobal_load_lds_dwordx4 %0, %1 offset:2048 sc1" ::"v"(v), "s"(sb), "s"(lds) : "memory");
  if constexpr (N == 4) asm volatile("s_mov_b32 m0, %2\n\ts_nop 0\n\tglobal_load_lds_dwordx4 %0, %1 sc1\n\tglobal_load_lds_dwordx4 %0, %1 offset:1024 sc1\n\tglobal_load_lds_dwordx4 %0, %1 offset:2048 sc1\n\tglobal_load_lds_dwordx4 %0, %1 offset:3072 sc1" ::"v"(v), "s"(sb), "s"(lds) : "memory");
}
template <int OFF> DI void lds_tr(u32x2& d, unsigned a) { asm volatile("ds_read_b64_tr_b16 %0, %1 offset:%2" : "=v"(d) : "v"(a), "i"(OFF) : "memory"); }
template <int O0, int O1> DI void lds_tr2(u32x4& d, unsigned a) {
  u32x2 lo, hi;
  asm volatile("ds_read_b64_tr_b16 %0, %2 offset:%3\n\tds_read_b64_tr_b16 %1, %2 offset:%4" : "=&v"(lo), "=&v"(hi) : "v"(a), "i"(O0), "i"(O1) : "memory");
  d = u32x4{lo[0], lo[1], hi[0], hi[1]};
}
template <int OFF> DI void lds_128(u32x4& d, unsigned a) { asm volatile("ds_read_b128 %0, %1 offset:%2" : "=v"(d) : "v"(a), "i"(OFF) : "memory"); }
template <int OFF> DI void lds_128f(f32x4& d, unsigned a) { asm volatile("ds_read_b128 %0, %1 offset:%2" : "=v"(d) : "v"(a), "i"(OFF) : "memory"); }
template <class T> DI void tie1(T& x) { asm volatile("" : "+v"(x)); }
template <class... T> DI void tie(T&... x) { (tie1(x), ...); }
template <class T> DI void tie1a(T& x) { asm volatile("" : "+a"(x)); }
template <class... T> DI void tiea(T&... x) { (tie1a(x), ...); }

constexpr int K0_MODWG = 2 * 24 * MSPLIT;
constexpr int K0_LNWG = 2 * NTILE;
constexpr int CODE_KB = 56;
constexpr int K0_PFWG = 2;
constexpr int K0_GRID = K0_LNWG + K0_MODWG / 2 + K0_PFWG;
template <int NT, int ROWS, bool DEV, bool PRE>
DI void mod_slab(const unsigned short* __restrict__ W, const unsigned short* __restrict__ cvec, int row0, int col0, float* __restrict__ out, float* sm, int tid,
                 u32x4 (&w)[ROWS * 8 / NT]  , unsigned c0, unsigned c1) {
  constexpr int RG = NT / 8, NLD = ROWS / RG;
  static_assert(RG % 16 == 0 && ROWS % RG == 0 && ROWS / 2 <= NT, "");
  const int cg = tid & 7, rg = tid >> 3;
  if constexpr (!PRE) {
    if (tid < ROWS / 2) { c0 = *(const unsigned*)(cvec + row0 + 2 * tid); c1 = *(const unsigned*)(cvec + D + row0 + 2 * tid); }
    const unsigned short* wp = W + (size_t)(row0 + rg) * (6 * D) + col0 + cg * 8;
#pragma unroll
    for (int i = 0; i < NLD; ++i) w[i] = __builtin_nontemporal_load((const u32x4*)(wp + (size_t)i * RG * (6 * D)));
  }
  float* sc = sm + RG * 128 + 64;
  if (tid < ROWS / 2) {
    auto silu = [](float v) { return v / (1.f + __expf(-v)); };
    const unsigned s0 = pk_bf16(silu(bf_lo(c0)), silu(bf_hi(c0))), s1 = pk_bf16(silu(bf_lo(c1)), silu(bf_hi(c1)));
    *(f32x2*)(sc + 2 * tid) = f32x2{bf_lo(s0), bf_hi(s0)};
    *(f32x2*)(sc + ROWS + 2 * tid) = f32x2{bf_lo(s1), bf_hi(s1)};
  }
  __syncthreads();
  float a0[8], a1[8];
#pragma unroll
  for (int j = 0; j < 8; ++j) { a0[j] = 0.f; a1[j] = 0.f; }
#pragma unroll
  for (int i = 0; i < NLD; ++i) {
    const float s0 = sc[rg + RG * i], s1 = sc[ROWS + rg + RG * i];
#pragma unroll
    for (int j = 0; j < 4; ++j) {
      const float lo = bf_lo(w[i][j]), hi = bf_hi(w[i][j]);
      a0[2 * j] = fmaf(s0, lo, a0[2 * j]); a0[2 * j + 1] = fmaf(s0, hi, a0[2 * j + 1]);
      a1[2 * j] = fmaf(s1, lo, a1[2 * j]); a1[2 * j + 1] = fmaf(s1, hi, a1[2 * j + 1]);
    }
  }
  f32x4* rp = (f32x4*)(sm + rg * 128 + (rg >= RG / 2 ? 16 : 0) + cg * 8);
  rp[0] = f32x4{a0[0], a0[1], a0[2], a0[3]}; rp[1] = f32x4{a0[4], a0[5], a0[6], a0[7]};
  rp[16] = f32x4{a1[0], a1[1], a1[2], a1[3]}; rp[17] = f32x4{a1[4], a1[5], a1[6], a1[7]};
  __syncthreads();
  if (tid < 256) {
    const int o = tid >> 1, hf = tid & 1;
    float s = 0.f;
#pragma unroll
    for (int r = 0; r < RG / 2; ++r) s += sm[(hf * (RG / 2) + r) * 128 + hf * 16 + o];
    s += __shfl_xor(s, 1);
    if (!hf) {
      if constexpr (DEV) __hip_atomic_store((GAS float*)out + (o >> 6) * 2304 + (o & 63), s, __ATOMIC_RELAXED, __HIP_MEMORY_SCOPE_AGENT);
      else out[(o >> 6) * 2304 + (o & 63)] = s;
    }
  }
}
DI float x16_sum(float v) { auto r = __builtin_amdgcn_permlane16_swap(__float_as_uint(v), __float_as_uint(v), false, false); return __uint_as_float(r[0]) + __uint_as_float(r[1]); }
__global__ __launch_bounds__(512) void k0_prep(const unsigned short* __restrict__ x_img, const unsigned short* __restrict__ x_txt,
                                               const unsigned short* __restrict__ cvec, const unsigned short* __restrict__ wmod_img,
                                               const unsigned short* __restrict__ wmod_txt, const char* __restrict__ wqkv_img,
                                               const char* __restrict__ wqkv_txt, char* __restrict__ ws) {
  __shared__ __attribute__((aligned(16))) float sm2[2][4096 + 64 + 384];
  const int bid = blockIdx.x;
  RT_IN(0); STAMP_DECL; STAMP(0);
  const bool is_ln = bid < K0_LNWG, is_mod = !is_ln & (bid < K0_LNWG + K0_MODWG / 2);
  const int tt = bid >> 1, half = bid & 1, b = tt >= TPB, i = tt - b * TPB;
  const int w = threadIdx.x >> 6, lane = threadIdx.x & 63, r = lane & 15, h = (lane >> 4) & 1, kk = lane >> 5;
  const int m = (bid - K0_LNWG) * 2 + (threadIdx.x >> 8), ksp = m & 3, type = (m >> 2) & 1, slab = m >> 3;
  const char* xa = sel(i < 32, (const char*)x_img + (unsigned)((b * NI + i * 32 + half * 16 + r) * D * 2), (const char*)x_txt + (unsigned)((b * NTX + half * 16 + r) * D * 2)) + (6 * w + kk) * 32 + 16 * h;
  const char* ma = (const char*)sel((bool)type, wmod_txt, wmod_img) + (unsigned)(((ksp * (D / MSPLIT) + ((threadIdx.x & 255) >> 3)) * (6 * D) + slab * 64 + (threadIdx.x & 7) * 8) * 2);
  const char* base0 = sel(is_mod, ma, sel(is_ln, xa, (const char*)x_img));
  const unsigned st0 = sel(is_mod, 32 * 6 * D * 2, 64);
  const int ci = ksp * (D / MSPLIT) + 2 * imin0(threadIdx.x & 255, D / MSPLIT / 2 - 1);
  const unsigned c0 = *(const GAS unsigned*)(cvec + ci), c1 = *(const GAS unsigned*)(cvec + D + ci);
  u32x4 v[6];
#pragma unroll
  for (int s = 0; s < 3; ++s) v[s] = __builtin_nontemporal_load((const GAS u32x4*)(base0 + s * st0));
  if (is_mod) {
#pragma unroll
    for (int s = 3; s < 6; ++s) v[s] = __builtin_nontemporal_load((const GAS u32x4*)(base0 + s * st0));
  }
  if (is_ln) {
    float* sm = sm2[0];
    STAMP(11);
    float sum = 0.f;
#pragma unroll
    for (int s = 0; s < 3; ++s)
#pragma unroll
      for (int j = 0; j < 4; ++j) sum += bf_lo(v[s][j]) + bf_hi(v[s][j]);
    const float mw = xhalf_sum(x16_sum(sum)) * (1.f / 96.f);
    STAMP(12);
    float sq = 0.f;
#pragma unroll
    for (int s = 0; s < 3; ++s)
#pragma unroll
      for (int j = 0; j < 4; ++j) { const float a = bf_lo(v[s][j]) - mw, c = bf_hi(v[s][j]) - mw; sq = fmaf(a, a, sq); sq = fmaf(c, c, sq); }
    sq = xhalf_sum(x16_sum(sq));
    if (lane < 16) *(f32x2*)(sm + (r * 8 + w) * 2) = f32x2{mw, sq};
    __syncthreads();
    f32x4 p[4];
#pragma unroll
    for (int q = 0; q < 4; ++q) p[q] = *(const f32x4*)(sm + r * 16 + q * 4);
    const float mean = ((p[0][0] + p[0][2]) + (p[1][0] + p[1][2]) + (p[2][0] + p[2][2]) + (p[3][0] + p[3][2])) * 0.125f;
    float m2 = 0.f, dd = 0.f;
#pragma unroll
    for (int q = 0; q < 4; ++q) { const float d0 = p[q][0] - mean, d1 = p[q][2] - mean; m2 += p[q][1] + p[q][3]; dd = fmaf(d0, d0, fmaf(d1, d1, dd)); }
    const float rstd = 1.f / sqrtf((m2 + 96.f * dd) * (1.f / D) + 1e-5f);
    char* dst = ws + WS_XN + (size_t)tt * TILE_B + (size_t)(6 * w + kk) * 1024 + (h * 32 + half * 16 + r) * 16;
#pragma unroll
    for (int s = 0; s < 3; ++s) {
      u32x4 o;
#pragma unroll
      for (int j = 0; j < 4; ++j) o[j] = xn_pack((bf_lo(v[s][j]) - mean) * rstd, (bf_hi(v[s][j]) - mean) * rstd);
      gst16(dst + s * 2048, o);
    }
    STAMP(13); WAIT_VM(0); STAMP(14); STAMP_OUT(0);
  } else if (is_mod) {
    mod_slab<256, D / MSPLIT, false, true>(wmod_img, cvec, ksp * (D / MSPLIT), slab * 64,
                              (float*)(ws + WS_MOD) + (size_t)(type * MSPLIT + ksp) * 2 * 2304 + slab * 64, sm2[threadIdx.x >> 8], threadIdx.x & 255, v, c0, c1);
    STAMP(5); WAIT_VM(0); STAMP(6); STAMP_OUT(0);
  } else {
    const int pw = bid - (K0_LNWG + K0_MODWG / 2);
    if (pw == 0 && threadIdx.x < F_N) *(unsigned*)(ws + WS_FLAG + threadIdx.x * 256) = 0u;
    if (pw == 1) {
      unsigned lo, hi;
      asm volatile("s_getpc_b64 s[20:21]\n\ts_add_u32 %0, s20, _Z6k_mainILi7EEv4Args@rel32@lo+4\n\ts_addc_u32 %1, s21, _Z6k_mainILi7EEv4Args@rel32@hi+12"
                   : "=s"(lo), "=s"(hi)::"s20", "s21", "scc");
      const char* code = (const char*)(((unsigned long long)hi << 32) | lo) + threadIdx.x * 16;
      u32x4 c[CODE_KB / 8];
#pragma unroll
      for (int i = 0; i < CODE_KB / 8; ++i) c[i] = *(const GAS u32x4*)(code + i * 8192);
#pragma unroll
      for (int i = 0; i < CODE_KB / 8; ++i) asm volatile("" ::"v"(c[i]));
    }
    asm volatile("" ::"v"(v[0]), "v"(v[1]), "v"(v[2]));
  }
  RT_OUT(0);
}

#define BAR() __builtin_amdgcn_s_barrier()
#define WAIT_LG(n) asm volatile("s_waitcnt lgkmcnt(%0)" ::"n"(n) : "memory")
constexpr int imin(int a, int b) { return a < b ? a : b; }
constexpr int imax(int a, int b) { return a > b ? a : b; }
constexpr int NSTG = 24;
constexpr int LDS_MAIN = 163840;
constexpr int ld_cnt(int k, int ns, int n) { return k == 0 ? 3 : imin(imin(imin(n, k + 12), ld_cnt(k - 1, ns, n) + 2), imax(ns, k - 1 + ns - 1)); }
DI void bars(int n) {
#pragma unroll 1
  for (int i = 0; i < n; ++i) BAR();
}
template <int N, int NS, int PER, bool PRE, class F> DI void ring_loader(F&& issue) {
  sfor<0, N>([&](auto K) {
    constexpr int k = decltype(K)::v;
    sfor<(k == 0 ? 0 : ld_cnt(k - 1, NS, N)), ld_cnt(k, NS, N)>(issue);
    if constexpr (PRE && k == 0) BAR();
    WAIT_VM(PER * (ld_cnt(k, NS, N) - k - 1)); BAR();
  });
}
#define T_L20 6
#define T_L2S 4
constexpr int ld2_cnt(int k, int ns, int n) { return k == 0 ? T_L20 : imin(imin(n, ld2_cnt(k - 1, ns, n) + T_L2S), imax(ns, k - 1 + ns - 1)); }
template <int N, int NS, int PER, int PAR, class F> DI void ring_loader2(F&& issue, bool pre) {
  sfor<0, N>([&](auto K) {
    constexpr int k = decltype(K)::v, c = ld2_cnt(k, NS, N);
    sfor<(k == 0 ? 0 : ld2_cnt(k - 1, NS, N)), c>([&](auto G) { if constexpr ((decltype(G)::v & 1) == PAR) issue(G); });
    if constexpr (k == 0) { if (pre) BAR(); }
    WAIT_VM(PER * ((c + 1 - PAR) / 2 - (k + 2 - PAR) / 2)); BAR();
  });
}
DI void glds2x2(unsigned v, const char* sb0, const char* sb1m  , unsigned lds) {
  asm volatile(
      "s_mov_b32 m0, %3\n\ts_nop 0\n\t"
      "global_load_lds_dwordx4 %0, %1 sc1\n\t"
      "global_load_lds_dwordx4 %0, %1 offset:1024 sc1\n\t"
      "global_load_lds_dwordx4 %0, %2 offset:2048 sc1\n\t"
      "global_load_lds_dwordx4 %0, %2 offset:3072 sc1"
      ::"v"(v), "s"(sb0), "s"(sb1m), "s"(lds) : "memory");
}
DI void flag_add(char* ws, int f) { __hip_atomic_fetch_add((GAS unsigned*)(ws + WS_FLAG + f * 256), 1u, __ATOMIC_RELAXED, __HIP_MEMORY_SCOPE_AGENT); }
#define T_NAP 3
template <bool FUSED, int NAP = T_NAP> DI void flag_wait(char* ws, int f  , unsigned want  ) {
  if constexpr (FUSED) {
    GAS unsigned* p = (GAS unsigned*)(ws + WS_FLAG + f * 256);
#pragma unroll 1
    for (unsigned spins = 0; spins < (1u << 20); ++spins) {
      if (__all(__hip_atomic_load(p, __ATOMIC_RELAXED, __HIP_MEMORY_SCOPE_AGENT) >= want)) break;
      __builtin_amdgcn_s_sleep(NAP);
    }
    asm volatile("" ::: "memory");
  }
}

DI void w_voff(unsigned (&voff)[5], int lane, unsigned pitch) {
#pragma unroll
  for (int i = 0; i < 5; ++i) {
    const unsigned o = i * 64 + lane, row = o / 20, cb = (o % 20) * 16;
    voff[i] = row * pitch + cb - (i < 4 ? i * 1024 : 0);
  }
}

struct Args {
  const char *x_img, *x_txt, *wqkv_img, *wqkv_txt, *wo_img, *wo_txt;
  const unsigned short *cvec, *wmod_img, *wmod_txt;
  char *ws, *out;
  unsigned task[256];
};

#define T_HD 3
#define T_LA 7
#define T_XR 2
#define HD0 1
constexpr int W_SLOT = 10240, P1_NW = 10, P1_NH = 6, HD = T_HD, PF_START = 10;
constexpr int x_iss(int hj) { return hj < 0 ? 4 : imin(24, imin(4 + T_XR * (hj + 1), hj + 1 + 2 * HD)); }
constexpr int P1_H = P1_NW * W_SLOT;
constexpr int P1_MV = P1_H + P1_NH * 8192;
DI void phase1(const Args& A, const char* A_code, char* smem, int wave, int lane STAMP_ARG) {
  char* ws = A.ws;
  const int bid = blockIdx.x, xcd = bid & 7, idx = bid >> 3;
  const int rowhalf = xcd & 1, colq = xcd >> 1, h = lane >> 5;
  const bool txt = (colq == 3) & (idx >= 24);
  const int ct = sel(txt, rowhalf * 8 + (idx - 24), colq * 4 + (idx >> 3));
  const int n0 = sel(ct < 14, ct * 160, 2144);
  const unsigned lds0 = (unsigned)(size_t)(LAS char*)smem;
  const unsigned l16 = lane * 16;
  const int j = wave & 3;
  const char* xb = ws + WS_XN + (unsigned)(sel(txt, 32, rowhalf * TPB + (idx & 7) * 4) * TILE_B + j * 1024);
  const char* W = sel(txt, A.wqkv_txt, A.wqkv_img) + n0 * 2;
  const unsigned mr = imin(threadIdx.x, 383);
  const char* mp = ws + WS_MOD + (unsigned)(sel(txt, MSPLIT * 2, rowhalf) * 2304 * 4);
  asm volatile("s_nop 4" ::"s"(xb), "s"(W), "s"(mp) : "memory");
  if (ct >= 15) return;
  u32x4 m[MSPLIT];
  auto ldm = [&](const char* q) {
    const unsigned src = (mr < 192 ? 192 + mr : mr - 192) * 16;
    sfor<0, MSPLIT>([&](auto K) { constexpr int k = decltype(K)::v; gld16<0>(m[k], src, q + k * 18432); });
  };
  auto stm1 = [&](int bb) {
    tie(m[0], m[1], m[2], m[3]);
    auto f = [](u32x4 v) { return __builtin_bit_cast(f32x4, v); };
    *(f32x4*)(smem + P1_MV + bb * 6144 + mr * 16) = (f(m[0]) + f(m[1])) + (f(m[2]) + f(m[3])) + (mr < 192 ? 1.f : 0.f);
  };
  auto stm = [&]() {
    if (wave < 6) {
      stm1(0);
      if (txt) { ldm(mp + 2304 * 4); WAIT_VM(0); stm1(1); }
    }
    WAIT_LG(0);
    BAR();
  };
  if (__builtin_expect(wave >= 8, 1)) {
    if (wave >= 10) {
      u32x4 junk = {0, 0, 0, 0};
      const char* wo = sel((bool)(bid & 1), A.wo_txt, A.wo_img) + (bid >> 1) * 9216;
      const char* wg = (const char*)sel((bool)(bid & 1), A.wmod_txt, A.wmod_img) + (unsigned)((bid >> 1) * 6 * 9216 + 3072);
      const char* src = uni(sel(wave == 10, wo, wg));
      if (wave == 11 && idx < CODE_KB / 2) {
        const char* code = A_code + idx * 2048;
        touch16<0>(junk, l16, code); touch16<1024>(junk, l16, code);
      }
      bars(PF_START);
#pragma unroll 1
      for (int i = 0; i < 9; ++i) {
        const unsigned o = i * 64 + lane, row = o / 96;
        touch16<0>(junk, sel(wave == 10, i * 1024 + l16, row * 9216 + (o - row * 96) * 16), src);
        BAR();
      }
      bars(NSTG + 1 - 9 - PF_START);
      WAIT_VM(0); tie(junk);
    } else {
      unsigned voff[5];
      w_voff(voff, lane, 4608);
      const char* p = W + (wave - 8) * 16 * 4608;
      const unsigned lds = lds0 + (wave - 8) * 5120;
      ring_loader<NSTG, P1_NW, 5, true>([&](auto G) { glds5w(voff, p, lds + (decltype(G)::v % P1_NW) * W_SLOT); p += 32 * 4608; });
    }
  } else if (wave >= 4) {
    const unsigned x1 = l16 + sel(txt, TPB, 1) * TILE_B, x2 = l16 + sel(txt, 0, 2) * TILE_B, x3 = l16 + sel(txt, TPB, 3) * TILE_B;
    u32x4 xr[12][4];
    auto ldx = [&](auto I, auto U) {
      constexpr int i = decltype(I)::v, u = decltype(U)::v;
      if constexpr (i < 12) { gld16<0>(xr[i][u], u ? x1 : l16, xb + i * 4096); gld16<0>(xr[i][u + 2], u ? x3 : x2, xb + i * 4096); }
    };
    if (wave < 6) ldm(mp);
    sfor<0, HD0>([&](auto I) { ldx(I, IC<0>{}); ldx(I, IC<1>{}); });
    WAIT_VM(4 * HD0);
    stm();
    ldx(IC<1>{}, IC<0>{}); ldx(IC<1>{}, IC<1>{});
    auto more_x = [&](auto HJ) {
      constexpr int hj = decltype(HJ)::v;
      sfor<x_iss(hj - 1), x_iss(hj)>([&](auto U) { constexpr int u = decltype(U)::v; ldx(IC<u / 2>{}, IC<u % 2>{}); });
    };
    const unsigned mva0 = lds0 + P1_MV + h * 32 + j * 64, mva1 = mva0 + sel(txt, 6144, 0);
    char* hw = smem + P1_H + ((j >> 1) * 2 + (j & 1)) * 4096 + l16;
    f32x4 mv[2][4];
    auto rdm = [&](auto I, auto U) {
      constexpr int i = decltype(I)::v, u = decltype(U)::v;
      const unsigned a = u ? mva1 : mva0;
      if constexpr (i < 12) { lds_128f<i * 256>(mv[u][0], a); lds_128f<i * 256 + 16>(mv[u][1], a); lds_128f<3072 + i * 256>(mv[u][2], a); lds_128f<3072 + i * 256 + 16>(mv[u][3], a); }
    };
    auto modw = [&](auto I, auto T) {
      constexpr int i = decltype(I)::v, t = decltype(T)::v, q = t & 1;
      const u32x4 x = xr[i][t];
      u32x4 o;
#pragma unroll
      for (int e = 0; e < 4; ++e)
        o[e] = mod_pair(x[e], mv[q][e >> 1][(e & 1) * 2], mv[q][2 + (e >> 1)][(e & 1) * 2], mv[q][e >> 1][(e & 1) * 2 + 1], mv[q][2 + (e >> 1)][(e & 1) * 2 + 1]);
      *(u32x4*)(hw + ((2 * i) % P1_NH) * 8192 + t * 1024) = o;
    };
    rdm(IC<0>{}, IC<0>{}); rdm(IC<0>{}, IC<1>{});
    sfor<0, 12>([&](auto I) {
      constexpr int i = decltype(I)::v, mvn = i < 11 ? 4 : 0;
      WAIT_VM(2 * (x_iss(2 * i - 1) - 2 * i - 1)); WAIT_LG(i ? 6 : 4);
      modw(I, IC<0>{}); modw(I, IC<2>{});
      if constexpr (i > 0) more_x(IC<2 * i>{});
      rdm(IC<i + 1>{}, IC<0>{});
      WAIT_LG(2 + mvn);
      if constexpr (i > 0) { if (i > 1 || j >= 2) BAR(); }
      WAIT_VM(2 * (x_iss(i ? 2 * i : -1) - 2 * i - 2));
      modw(I, IC<1>{}); modw(I, IC<3>{});
      if constexpr (i > 0) more_x(IC<2 * i + 1>{});
      if constexpr (i == 0) WAIT_LG(0);
      rdm(IC<i + 1>{}, IC<1>{});
      WAIT_LG(2 + mvn);
      BAR();
      if constexpr (i == 0) { more_x(IC<0>{}); more_x(IC<1>{}); }
    });
    WAIT_LG(0);
    BAR();
    if (j < 2) BAR();
  } else {
    ldm(mp); WAIT_VM(0); stm();
    STAMP(2);
    f32x16 acc[5];
    const int blk = (lane >> 4) & 1, q4 = (lane & 15) >> 2, p = lane & 3;
    unsigned rw = lds0 + (8 * h + q4) * 320 + blk * 32 + p * 8, rw1 = rw + 5 * W_SLOT;
    unsigned rh = lds0 + P1_H + wave * 1024 + l16;
    tie(rw, rw1, rh);
    constexpr int LA = T_LA;
    u32x4 wf[5 * KS], hh[KS];
    auto rdw = [&](auto N) {
      constexpr int n = decltype(N)::v, s = n / 5, c = n % 5, slot = (s >> 1) % P1_NW, so = (slot % 5) * W_SLOT + (s & 1) * 5120;
      if constexpr (s < KS) {
        if constexpr (c == 0 && (s & 1) == 0 && s > 0) BAR();
        if constexpr (c == 0) lds_128<((s >> 1) % P1_NH) * 8192 + (s & 1) * 4096>(hh[s], rh);
        lds_tr2<so + c * 64, so + c * 64 + 1280>(wf[n], slot < 5 ? rw : rw1);
      }
    };
    STAMP(3);
    BAR();
    STAMP(4);
    sfor<0, LA>(rdw);
    sfor<0, 5 * KS>([&](auto N) {
      constexpr int n = decltype(N)::v, s = n / 5, c = n % 5;
      constexpr int hi = imin(n + LA - 1, 5 * KS - 1);
      WAIT_LG(2 * (hi - n) + (hi / 5 - n / 5));
      mfma_v<s == 0>(acc[c], wf[n], hh[s]);
      rdw(IC<n + LA>{});
    });
    asm volatile("s_nop 15" : "+v"(acc[0]), "+v"(acc[1]), "+v"(acc[2]), "+v"(acc[3]), "+v"(acc[4]));
    STAMP(7);
    const int tt = sel(txt, (wave & 1) * TPB + 32, rowhalf * TPB + (idx & 7) * 4 + wave);
    if (!(txt && wave >= 2)) {
#pragma unroll
      for (int c = 0; c < 5; ++c) {
        const int f0 = n0 + c * 32;
        if (ct == 14 && c < 3) continue;
        const int mat = f0 / D, st = (f0 % D) >> 4;
        char* dst = ws + WS_QKV + ((size_t)mat * NTILE + tt) * TILE_B + st * 1024 + l16;
        gst16(dst, pack8(acc[c], 0));
        gst16(dst + 1024, pack8(acc[c], 1));
      }
    }
    WAIT_VM(0);
    STAMP(8);
  }
  BAR();
  if (threadIdx.x == 0) {
    const int rb = sel(txt, 16, rowhalf * 8 + (idx & 7)), m0 = (ct * 160) / D, m1 = imin(ct * 160 + 159, 2303) / D;
    flag_add(ws, F_QKV + rb * 3 + m0);
    if (m1 != m0) flag_add(ws, F_QKV + rb * 3 + m1);
  }
}

#define T_RA2 10
constexpr int P2_NS = 14, P2_RA = T_RA2;
template <int MODE>
DI void p2_consumer(f32x16 (&acc)[5], const char* priv, unsigned lds0, int lane STAMP_ARG) {
  const int h = lane >> 5, blk = (lane >> 4) & 1, q4 = (lane & 15) >> 2, p = lane & 3;
  const unsigned l16 = lane * 16;
  constexpr int PW = MODE == 2 ? 1 : 2;
  u32x4 xr[KS], wf[2][5];
  auto ldx = [&](auto S) { constexpr int s = decltype(S)::v; if constexpr (s < KS) gld16_sc1<(s & 3) * 1024>(xr[s], l16, priv + (s >> 2) * 4096); };
  sfor<0, P2_RA>(ldx);
  unsigned rw = MODE == 1 ? lds0 + (4 * h + q4) * 320 + blk * 32 + p * 8 : lds0 + l16, rw1 = rw + 5 * W_SLOT, rw2 = rw + 10 * W_SLOT;
  tie(rw, rw1, rw2);
  auto rdw = [&](auto S, auto C) {
    constexpr int s = decltype(S)::v, c = decltype(C)::v, cur = s & 1, slot = (s >> 1) % P2_NS, so = (slot % 5) * W_SLOT + (s & 1) * 5120;
    if constexpr (s < KS) {
      const unsigned a = slot < 5 ? rw : (slot < 10 ? rw1 : rw2);
      if constexpr (MODE == 2) lds_128<so + c * 1024>(wf[cur][c], a);
      else lds_tr2<so + c * 64, so + c * 64 + 2560>(wf[cur][c], a);
    }
  };
  STAMP(11);
  BAR();
  STAMP(12);
  sfor<0, 5>([&](auto C) { rdw(IC<0>{}, C); });
  sfor<0, KS>([&](auto S) {
    constexpr int s = decltype(S)::v, cur = s & 1;
    constexpr bool more = s + 1 < KS;
    if constexpr ((s & 1) && more) BAR();
    WAIT_VM(imin(P2_RA - 1, KS - 1 - s));
    sfor<0, 5>([&](auto C) {
      constexpr int c = decltype(C)::v;
      WAIT_LG((4 - c) * PW + (more ? c * PW : 0));
      if constexpr (MODE == 1) mfma_v<s == 0>(acc[c], xr[s], wf[cur][c]);
      else mfma_v<s == 0>(acc[c], wf[cur][c], xr[s]);
      rdw(IC<s + 1>{}, C);
      if constexpr (c == 2) ldx(IC<s + P2_RA>{});
    });
  });
  asm volatile("s_nop 15" : "+v"(acc[0]), "+v"(acc[1]), "+v"(acc[2]), "+v"(acc[3]), "+v"(acc[4]));
  STAMP(15);
}
template <int NT> DI void p2_softmax(f32x16 (&acc)[5], char* dst  , float* st, int own0, int lane) {
  float m = -3.0e38f;
#pragma unroll
  for (int c = 0; c < NT; ++c)
    if (c >= own0) {
#pragma unroll
      for (int r = 0; r < 16; ++r) m = fmaxf(m, acc[c][r]);
    }
  m = xhalf_max(m);
  const float mc = -m * SM_C;
  float l = 0.f;
#pragma unroll
  for (int c = 0; c < NT; ++c)
    if (c >= own0) {
#pragma unroll
      for (int r = 0; r < 16; ++r) { const float pv = __builtin_amdgcn_exp2f(fmaf(acc[c][r], SM_C, mc)); acc[c][r] = pv; l += pv; }
      gst16(dst + c * 2048, pack8(acc[c], 0));
      gst16(dst + c * 2048 + 1024, pack8(acc[c], 1));
    }
  l = xhalf_sum(l);
  __hip_atomic_store((GAS float*)st + lane, lane < 32 ? m : l, __ATOMIC_RELAXED, __HIP_MEMORY_SCOPE_AGENT);
}
template <bool FUSED> DI void phase2(const Args& A, unsigned task, char* smem, int wave, int lane STAMP_ARG) {
  char* ws = A.ws;
  const int b = (blockIdx.x >> 2) & 1, kind = task & 7;
  const int pb = (task >> 3) & 15;
  const int sc = (task >> 7) & 15;
  const unsigned lds0 = (unsigned)(size_t)(LAS char*)smem;
  const unsigned l16 = lane * 16;
  const bool isS = kind == 1;
  const int kt0 = sel(sc < 6, sc * 5, 28);
  const int n0 = sel(sc < 4, sc * 160, 608);
  if (kind == 0) return;
  if (kind == 4) {
    u32x4 w[8];
    mod_slab<768, D, true, false>(pb ? A.wmod_txt : A.wmod_img, A.cvec, 0, 1536 + sc * 64, (float*)(ws + WS_MOD) + (size_t)pb * MSPLIT * 2 * 2304 + 1536 + sc * 64, (float*)smem, threadIdx.x, w, 0u, 0u);
    WAIT_VM(0);
    BAR();
    STAMP(17);
    if (threadIdx.x == 0) flag_add(ws, F_GATE + pb * 12 + sc);
    return;
  }
  if (kind == 3) {
    const int qt = b * TPB + 32, own0 = sc < 6 ? 0 : 2;
    float* xm = (float*)(smem + KS * 1024);
    if (wave == 5) flag_wait<FUSED>(ws, sel(lane & 2, F_QKV + 16 * 3, F_QKV + 1 + 3 * sel(lane & 1, sc < 6 ? b * 8 + ((kt0 + 4) >> 2) : 16, b * 8 + (kt0 >> 2))), sel(lane & 2, 5u, 6u));
    BAR();
    if (wave >= 8) {
      const char* q = uni(ws + WS_QKV + (unsigned)(qt * TILE_B + (wave - 8) * 12288));
      asm volatile("s_nop 4" ::"s"(q) : "memory");
      sfor<0, 3>([&](auto I) { constexpr int i = decltype(I)::v; gldsN<4>(l16, q + i * 4096, lds0 + (wave - 8) * 12288 + i * 4096); });
      WAIT_VM(0);
      bars(3);
    } else if (wave >= 5) bars(3);
    else {
      const int kt = kt0 + wave;
      const char* kp = uni(ws + WS_QKV + (unsigned)((NTILE + b * TPB + kt) * TILE_B));
      asm volatile("s_nop 4" ::"s"(kp) : "memory");
      constexpr int RA = 24;
      u32x4 kf[KS], qf[KS];
      auto ldk = [&](auto S) { constexpr int s = decltype(S)::v; if constexpr (s < KS) gld16_sc1<(s & 3) * 1024>(kf[s], l16, kp + (s >> 2) * 4096); };
      auto rdq = [&](auto S) { constexpr int s = decltype(S)::v; if constexpr (s < KS) lds_128<s * 1024>(qf[s], lds0 + l16); };
      sfor<0, RA>(ldk);
      BAR();
      sfor<0, 4>(rdq);
      f32x16 acc;
      sfor<0, KS>([&](auto S) {
        constexpr int s = decltype(S)::v;
        WAIT_VM(imin(RA - 1, KS - 1 - s)); WAIT_LG(imin(3, KS - 1 - s));
        mfma_v<s == 0>(acc, kf[s], qf[s]);
        rdq(IC<s + 4>{}); ldk(IC<s + RA>{});
      });
      asm volatile("s_nop 15" : "+v"(acc));
      float m = acc[0];
#pragma unroll
      for (int r = 1; r < 16; ++r) m = fmaxf(m, acc[r]);
      m = xhalf_max(m);
      xm[wave * 32 + (lane & 31)] = wave >= own0 ? m : -3.0e38f;
      WAIT_LG(0);
      BAR();
#pragma unroll
      for (int w = 0; w < 5; ++w) m = w ? fmaxf(m, xm[w * 32 + (lane & 31)]) : xm[lane & 31];
      const float mc = -m * SM_C;
      float l = 0.f;
#pragma unroll
      for (int r = 0; r < 16; ++r) { const float pv = __builtin_amdgcn_exp2f(fmaf(acc[r], SM_C, mc)); acc[r] = pv; l += pv; }
      if (wave >= own0) {
        char* dst = ws + WS_P + (size_t)qt * PT_B + (size_t)kt * 2048 + l16;
        gst16(dst, pack8(acc, 0)); gst16(dst + 1024, pack8(acc, 1));
      }
      l = xhalf_sum(l);
      xm[160 + wave * 32 + (lane & 31)] = wave >= own0 ? l : 0.f;
      WAIT_LG(0);
      BAR();
      if (wave == 0) {
        l = 0.f;
#pragma unroll
        for (int w = 0; w < 5; ++w) l += xm[160 + w * 32 + (lane & 31)];
        __hip_atomic_store((GAS float*)(ws + WS_ST) + ((size_t)qt * NCH + sc) * 64 + lane, lane < 32 ? m : l, __ATOMIC_RELAXED, __HIP_MEMORY_SCOPE_AGENT);
      }
      WAIT_VM(0);
    }
    BAR();
    STAMP(16);
    if (threadIdx.x == 0) flag_add(ws, F_P + b * 9 + 8);
    return;
  }
  if (__builtin_expect(wave >= 4, 1)) {
    if (wave >= 8) {
      if (wave == 8) flag_wait<FUSED>(ws, sel((lane & 2) || !isS, F_QKV + sel(pb < 8, b * 8 + pb, 16) * 3 + sel(isS, 0, 2),
                                              F_QKV + 1 + 3 * sel(lane & 1, sc < 6 ? b * 8 + ((kt0 + 4) >> 2) : 16, b * 8 + (kt0 >> 2))), sel((lane & 2) && isS, 5u, 6u));
      bars(NSTG + 1);
    } else {
      unsigned voff[5];
      w_voff(voff, lane, 1536);
      if (isS) {
#pragma unroll
        for (int i = 0; i < 5; ++i) voff[i] = i * TILE_B + l16 - (i < 4 ? i * 1024 : 0);
      }
      const unsigned stride = __builtin_amdgcn_readfirstlane(sel(isS, 2048, 32 * 1536)), lds = lds0 + (wave & 1) * 5120;
      const char* p = uni(sel(isS, (const char*)ws + WS_QKV + (unsigned)((NTILE + b * TPB + kt0) * TILE_B + (wave & 1) * 1024), A.wo_img + (n0 * 2 + (wave & 1) * 16 * 1536)));
      p = sadd(p, ((wave >> 1) & 1) * stride);
      asm volatile("s_nop 4" ::"s"(p), "s"(stride) : "memory");
      auto issue = [&](auto G) { glds5(voff, p, lds + (decltype(G)::v % P2_NS) * W_SLOT); p = sadd(p, 2 * stride); };
      if (isS) BAR();
      if (wave < 6) ring_loader2<NSTG, P2_NS, 5, 0>(issue, !isS); else ring_loader2<NSTG, P2_NS, 5, 1>(issue, !isS);
    }
  } else {
    f32x16 acc[5];
    const int tl = sel(pb < 8, pb * 4 + wave, 32);
    const bool active = pb < 8 || wave == 0;
    const char* priv = uni(ws + WS_QKV + (unsigned)((sel(isS, 0, 2 * NTILE) + b * TPB + tl) * TILE_B));
    asm volatile("s_nop 4" ::"s"(priv) : "memory");
    BAR();
    if (isS) {
      const int qt = b * TPB + tl;
      p2_consumer<2>(acc, priv, lds0, lane STAMP_PASS);
      p2_softmax<5>(acc, ws + WS_P + (size_t)qt * PT_B + (size_t)kt0 * 2048 + l16, (float*)(ws + WS_ST) + ((size_t)qt * NCH + sc) * 64, sc < 6 ? 0 : 2, lane);
    } else {
      p2_consumer<1>(acc, priv, lds0, lane STAMP_PASS);
      if (active) {
        char* dst = ws + WS_VP + ((size_t)b * 24 + (n0 >> 5)) * PT_B + (size_t)tl * 2048 + l16;
#pragma unroll
        for (int c = 0; c < 5; ++c) {
          if (sc == 4 && c == 0) continue;
          gst16(dst + (size_t)c * PT_B, pack8(acc[c], 0));
          gst16(dst + (size_t)c * PT_B + 1024, pack8(acc[c], 1));
        }
      }
    }
    STAMP(16);
    WAIT_VM(0);
    STAMP(17);
  }
  BAR();
  if (threadIdx.x == 0) flag_add(ws, sel(isS, F_P + b * 9 + pb, F_VP + b * 5 + sc));
}

constexpr int P3_RA = 11;
constexpr int NSTG3 = 22;
#define T_L3 6
#define T_L30 2
constexpr int ld3_cnt(int k) { return k == 0 ? T_L30 : imin(NSTG3, k + T_L3); }
DI void fma4(f32x16& acc, const f32x16& t, float f, int e) {
  asm volatile("v_fmac_f32 %0, %4, %8\n\tv_fmac_f32 %1, %5, %8\n\tv_fmac_f32 %2, %6, %8\n\tv_fmac_f32 %3, %7, %8"
               : "+v"(acc[e]), "+v"(acc[e + 1]), "+v"(acc[e + 2]), "+v"(acc[e + 3]) : "v"(t[e]), "v"(t[e + 1]), "v"(t[e + 2]), "v"(t[e + 3]), "v"(f));
}
DI void chunk_scales(float (&f)[NCH], const float (&mm)[NCH], const float (&ll)[NCH]) {
  float m = mm[0];
#pragma unroll
  for (int c = 1; c < NCH; ++c) m = fmaxf(m, mm[c]);
  float l = 0.f;
#pragma unroll
  for (int c = 0; c < NCH; ++c) { f[c] = __builtin_amdgcn_exp2f((mm[c] - m) * SM_C); l = fmaf(f[c], ll[c], l); }
  const float inv = 1.f / l;
#pragma unroll
  for (int c = 0; c < NCH; ++c) f[c] *= inv;
}
DI void ld_stats(float (&mm)[NCH], float (&ll)[NCH], const char* stp, int r) {
  sfor<0, NCH>([&](auto C) { constexpr int c = decltype(C)::v; gld4<c * 256>(mm[c], r * 4, stp); gld4<c * 256 + 128>(ll[c], r * 4, stp); });
}
DI void tie_stats(float (&mm)[NCH], float (&ll)[NCH]) {
#pragma unroll
  for (int c = 0; c < NCH; ++c) tie(mm[c], ll[c]);
}
template <bool FUSED> DI void phase3(const Args& A, unsigned task, char* smem, int wave, int lane STAMP_ARG) {
  char* ws = A.ws;
  const int b = (blockIdx.x >> 2) & 1, kind = (task >> 11) & 3;
  const int r = lane & 31, h = lane >> 5;
  const unsigned lds0 = (unsigned)(size_t)(LAS char*)smem;
  const unsigned l16 = lane * 16;
  if (kind == 0) return;
  if (kind == 1) {
    const int qb = (task >> 13) & 7, dp = (task >> 16) & 31;
    if (wave >= 7) return;
    if (wave == 6) {
      flag_wait<FUSED>(ws, sel(lane & 2, sel(lane & 1, F_GATE + dp, F_P + b * 9 + qb), F_VP + b * 5 + imin((dp * 64 + (lane & 1) * 63) / 160, 4)),
                       sel(lane & 2, sel(lane & 1, 1u, 7u), 9u));
      BAR();
      return;
    }
    if (__builtin_expect(wave >= 4, 1)) {
      const char* p = uni(ws + WS_VP + (unsigned)((b * 24 + dp * 2 + (wave - 4)) * PT_B));
      const unsigned lds = lds0 + (wave - 4) * (KSK * 1024);
      asm volatile("s_nop 4" ::"s"(p) : "memory");
      BAR();
      sfor<0, NSTG3>([&](auto K) {
        constexpr int k = decltype(K)::v;
        sfor<(k == 0 ? 0 : ld3_cnt(k - 1)), ld3_cnt(k)>([&](auto G) { constexpr int g = decltype(G)::v; gldsN<3>(l16, p + g * 3072, lds + g * 3072); });
        WAIT_VM(3 * (ld3_cnt(k) - k - 1)); BAR();
      });
      return;
    }
    const int qt = b * TPB + qb * 4 + wave;
    const char* pb = uni(ws + WS_P + (unsigned)(qt * PT_B));
    const char* stp = uni(ws + WS_ST + (unsigned)(qt * NCH * 256));
    asm volatile("s_nop 4" ::"s"(pb), "s"(stp) : "memory");
    BAR();
    u32x4 pf[KSK];
    auto ldp = [&](auto S) { constexpr int s = decltype(S)::v; if constexpr (s < KSK) gld16_sc1<(s & 3) * 1024>(pf[s], l16, pb + (s >> 2) * 4096); };
    float mm[NCH], ll[NCH], f[NCH];
    sfor<0, 3>(ldp);
    ld_stats(mm, ll, stp, r);
    sfor<3, P3_RA>(ldp);
    f32x16 acc[2], tmp[2];
    acc[0] = f32x16{}; acc[1] = f32x16{};
    tie(acc[0], acc[1]);
    u32x4 vf[2][2];
    unsigned rv0 = lds0 + l16, rv1 = rv0 + KSK * 1024;
    tie(rv0, rv1);
    auto rdv = [&](auto S, auto T) {
      constexpr int s = decltype(S)::v, t = decltype(T)::v;
      if constexpr (s < 64) lds_128<s * 1024>(vf[s & 1][t], t ? rv1 : rv0);
      else if constexpr (s < KSK) lds_128<(s - 2) * 1024>(vf[s & 1][t], (t ? rv1 : rv0) + 2048);
    };
    u32x4 xv[4];
    float gl;
    const unsigned orow = ((b * NI + (qb * 4 + wave) * 32 + (lane >> 3)) * D + dp * 64) * 2 + (lane & 7) * 16;
    STAMP(19);
    BAR();
    STAMP(20);
    rdv(IC<0>{}, IC<0>{}); rdv(IC<0>{}, IC<1>{}); rdv(IC<1>{}, IC<0>{}); rdv(IC<1>{}, IC<1>{});
    sfor<0, KSK>([&](auto S) {
      constexpr int s = decltype(S)::v, cur = s & 1, c = s / 10, cs = s % 10;
      if constexpr ((s + 2) % 3 == 0 && s + 2 < KSK) BAR();
      WAIT_VM(imin(P3_RA - 1, KSK - 1 - s) + (s < 3 ? 2 * NCH : 0) + (s > KSK - 1 - P3_RA ? 5 : 0));
      sfor<0, 2>([&](auto T) {
        constexpr int t = decltype(T)::v;
        WAIT_LG((1 - t) + (s + 1 < KSK ? 2 : 0) + (s + 2 < KSK ? t : 0));
        mfma_v<cs == 0>(tmp[t], vf[cur][t], pf[s]);
        rdv(IC<s + 2>{}, T);
        if constexpr (t == 0) {
          ldp(IC<s + P3_RA>{});
          if constexpr (s == KSK - 1 - P3_RA) {
            sfor<0, 4>([&](auto E) { constexpr int e = decltype(E)::v; gld16<0>(xv[e], orow, A.x_img + e * 8 * D * 2); });
            gld4<0>(gl, lane * 4, ws + WS_MOD + (unsigned)((b * 2304 + 1536 + dp * 64) * 4));
          }
        }
      });
      if constexpr (cs == 9) {
        asm volatile("s_nop 9" : "+v"(tmp[0]));
        fma4(acc[0], tmp[0], f[c], 0); fma4(acc[0], tmp[0], f[c], 4); fma4(acc[0], tmp[0], f[c], 8); fma4(acc[0], tmp[0], f[c], 12);
        fma4(acc[1], tmp[1], f[c], 0); fma4(acc[1], tmp[1], f[c], 4); fma4(acc[1], tmp[1], f[c], 8); fma4(acc[1], tmp[1], f[c], 12);
      }
      if constexpr (s == 4) { tie_stats(mm, ll); chunk_scales(f, mm, ll); SBAR(); }
    });
    asm volatile("s_nop 15" : "+v"(tmp[0]), "+v"(tmp[1]));
    WAIT_VM(0); tie(xv[0], xv[1], xv[2], xv[3], gl);
    BAR();
    STAMP(21);
    {
      char* tb = smem + wave * 8192;
      *(float*)(smem + 32768 + wave * 256 + lane * 4) = gl;
#pragma unroll
      for (int e = 0; e < 8; ++e) {
        const int t = e >> 2, gq = e & 3;
        f32x4 y;
#pragma unroll
        for (int k = 0; k < 4; ++k) y[k] = fmaf(tmp[t][4 * gq + k], f[6], acc[t][4 * gq + k]);
        *(f32x4*)(tb + r * 256 + (((2 * e + h) ^ (r & 15)) * 16)) = y;
      }
      const f32x4 g0 = *(const f32x4*)(smem + 32768 + wave * 256 + (lane & 7) * 32), g1 = *(const f32x4*)(smem + 32768 + wave * 256 + (lane & 7) * 32 + 16);
#pragma unroll
      for (int e = 0; e < 4; ++e) {
        const int row = e * 8 + (lane >> 3), c2 = (lane & 7) * 2;
        const f32x4 y0 = *(const f32x4*)(tb + row * 256 + ((c2 ^ (row & 15)) * 16));
        const f32x4 y1 = *(const f32x4*)(tb + row * 256 + (((c2 + 1) ^ (row & 15)) * 16));
        u32x4 o = {pk_bf16(fmaf(g0[0], y0[0], bf_lo(xv[e][0])), fmaf(g0[1], y0[1], bf_hi(xv[e][0]))), pk_bf16(fmaf(g0[2], y0[2], bf_lo(xv[e][1])), fmaf(g0[3], y0[3], bf_hi(xv[e][1]))),
                   pk_bf16(fmaf(g1[0], y1[0], bf_lo(xv[e][2])), fmaf(g1[1], y1[1], bf_hi(xv[e][2]))), pk_bf16(fmaf(g1[2], y1[2], bf_lo(xv[e][3])), fmaf(g1[3], y1[3], bf_hi(xv[e][3])))};
        gst16(A.out + orow + (size_t)e * 8 * D * 2, o);
      }
    }
    STAMP(22);
  } else {
    if (wave >= 8) return;
    const int T = (task >> 16) & 31;
    const int qt = b * TPB + 32;
    constexpr int LV = 0, LW = 8 * 10240, LR = LW + 768 * 64;
    if (wave == 7) {
      flag_wait<FUSED, 16>(ws, F_P + b * 9 + 8, 7u);
      flag_wait<FUSED>(ws, sel(lane < 9, F_QKV + 2 + 3 * sel(lane < 8, b * 8 + lane, 16), F_GATE + 12 + (T >> 1)), sel(lane < 9, 6u, 1u));
    }
    BAR();
    STAMP(1);
    const int g0 = sel(wave < 6, wave * 5, 28);
    const char* vsrc = uni(ws + WS_QKV + (unsigned)((2 * NTILE + b * TPB + g0) * TILE_B + T * 2048));
    const char* pA = uni(ws + WS_P + (unsigned)(qt * PT_B + g0 * 2048));
    const char* stp = uni(ws + WS_ST + (unsigned)(qt * NCH * 256));
    const char* wsrc = uni(A.wo_txt + (unsigned)(96 * wave * 1536 + T * 64));
    asm volatile("s_nop 4" ::"s"(vsrc), "s"(pA), "s"(stp), "s"(wsrc) : "memory");
    const unsigned lv = lds0 + LV + wave * 10240;
    sfor<0, 5>([&](auto K) { constexpr int kt = decltype(K)::v; gldsN<2>(l16, vsrc + kt * TILE_B, lv + kt * 2048); });
    u32x4 pa[10];
    sfor<0, 10>([&](auto S) { constexpr int s = decltype(S)::v; gld16_sc1<(s & 3) * 1024>(pa[s], l16, pA + (s >> 2) * 4096); });
    float mm[NCH], ll[NCH], f[NCH];
    ld_stats(mm, ll, stp, r);
    {
      const unsigned wv = (lane >> 2) * 1536 + (lane & 3) * 16;
      sfor<0, 6>([&](auto I) { constexpr int i = decltype(I)::v; gldsN<1>(wv, wsrc + i * 16 * 1536, lds0 + LW + (96 * wave + 16 * i) * 64); });
    }
    const int blk = (lane >> 4) & 1, q4 = (lane & 15) >> 2, p = lane & 3;
    const unsigned va = lv + blk * 1024 + ((p & 1) * 32 + 4 * h + q4) * 16 + (p >> 1) * 8;
    WAIT_VM(6);
    STAMP(10);
    tie_stats(mm, ll);
    tie(pa[0], pa[1], pa[2], pa[3], pa[4], pa[5], pa[6], pa[7], pa[8], pa[9]);
    f32x16 ta = f32x16{};
    {
      u32x4 v[10];
      auto rdv = [&](auto S) { constexpr int s = decltype(S)::v, o = (s >> 1) * 2048 + (s & 1) * 256; lds_tr2<o, o + 128>(v[s], va); };
      sfor<0, 7>(rdv);
      chunk_scales(f, mm, ll);
      const int skip = wave >= 6 ? 4 : 0;
      sfor<0, 10>([&](auto S) {
        constexpr int s = decltype(S)::v;
        WAIT_LG(2 * (imin(s + 7, 10) - s - 1)); tie(v[s]);
        if constexpr (s + 7 < 10) rdv(IC<s + 7>{});
        if (s >= skip) ta = mfma(v[s], pa[s], ta);
      });
    }
    float fa = f[0];
#pragma unroll
    for (int w = 1; w < 7; ++w) if (wave == w) fa = f[w];
    f32x4* red = (f32x4*)(smem + LR);
    if (wave < 7) {
#pragma unroll
      for (int e = 0; e < 4; ++e) red[(wave * 4 + e) * 64 + lane] = f32x4{ta[4 * e], ta[4 * e + 1], ta[4 * e + 2], ta[4 * e + 3]} * fa;
    }
    u32x2 xv; u32x4 gq;
    gld8<0>(xv, (r * D + 4 * h + (wave & 3) * 8) * 2, uni(A.x_txt + (unsigned)((b * NTX * D + T * 32) * 2)));
    gld16_sc1<0>(gq, h * 16 + (wave & 3) * 32, uni(ws + WS_MOD + (unsigned)(((MSPLIT * 2 + b) * 2304 + 1536 + T * 32) * 4)));
    WAIT_LG(0);
    BAR();
    if (wave < 2) {
      f32x4 s0 = red[(2 * wave) * 64 + lane], s1 = red[(2 * wave + 1) * 64 + lane];
#pragma unroll
      for (int w = 1; w < 7; ++w) { s0 += red[(w * 4 + 2 * wave) * 64 + lane]; s1 += red[(w * 4 + 2 * wave + 1) * 64 + lane]; }
      gst16(ws + WS_AT + (size_t)b * TILE_B + T * 2048 + wave * 1024 + l16, u32x4{pk_bf16(s0[0], s0[1]), pk_bf16(s0[2], s0[3]), pk_bf16(s1[0], s1[1]), pk_bf16(s1[2], s1[3])});
      WAIT_VM(0);
      STAMP(23);
      if (lane == 0) flag_add(ws, F_TXT + b);
    }
    u32x4 wq[6];
    WAIT_VM(0); tie(xv, gq);
    {
      const unsigned wa = lds0 + LW + (96 * wave + 4 * h + q4) * 64 + blk * 32 + p * 8;
      sfor<0, 6>([&](auto S) { constexpr int s = decltype(S)::v; lds_tr2<s * 1024, s * 1024 + 512>(wq[s], wa); });
    }
    if (wave == 7) flag_wait<true>(ws, F_TXT + b, 48u);
    WAIT_LG(0);
    BAR();
    u32x4 af[6];
    const char* ab = uni(ws + WS_AT + (unsigned)(b * TILE_B + wave * 6144));
    asm volatile("s_nop 4" ::"s"(ab) : "memory");
    sfor<0, 6>([&](auto S) { constexpr int s = decltype(S)::v; gld16_sc1<(s & 3) * 1024>(af[s], l16, ab + (s >> 2) * 4096); });
    WAIT_VM(0);
    tie(af[0], af[1], af[2], af[3], af[4], af[5], wq[0], wq[1], wq[2], wq[3], wq[4], wq[5]);
    f32x16 tb = f32x16{};
#pragma unroll
    for (int s = 0; s < 6; ++s) tb = mfma(wq[s], af[s], tb);
#pragma unroll
    for (int e = 0; e < 4; ++e) red[(wave * 4 + e) * 64 + lane] = f32x4{tb[4 * e], tb[4 * e + 1], tb[4 * e + 2], tb[4 * e + 3]};
    WAIT_LG(0);
    BAR();
    if (wave < 4) {
      f32x4 o = red[wave * 64 + lane];
#pragma unroll
      for (int w = 1; w < 8; ++w) o += red[(w * 4 + wave) * 64 + lane];
      const f32x4 gt = __builtin_bit_cast(f32x4, gq);
      u32x2 ov = {pk_bf16(fmaf(gt[0], o[0], bf_lo(xv[0])), fmaf(gt[1], o[1], bf_hi(xv[0]))), pk_bf16(fmaf(gt[2], o[2], bf_lo(xv[1])), fmaf(gt[3], o[3], bf_hi(xv[1])))};
      *(u32x2*)(A.out + ((size_t)2 * NI * D + ((size_t)b * NTX + r) * D + T * 32 + 4 * h + wave * 8) * 2) = ov;
    }
  }
}

template <int PH> __global__ __launch_bounds__(768, 3) void k_main(Args A) {
  extern __shared__ __attribute__((aligned(16))) char smem[];
  STAMP_DECL; STAMP(0); RT_IN(1);
  const int wave = __builtin_amdgcn_readfirstlane(threadIdx.x >> 6), lane = threadIdx.x & 63;
  const unsigned task = A.task[blockIdx.x];
  const char* code;
  asm volatile("s_getpc_b64 %0" : "=s"(code));
  if constexpr (PH & 1) phase1(A, code, smem, wave, lane STAMP_PASS);
  STAMP(9);
  RT_MID(0);
  if constexpr (PH & 2) phase2<PH == 7>(A, task, smem, wave, lane STAMP_PASS);
  STAMP(18);
  RT_MID(1);
  if constexpr (PH & 4) phase3<PH == 7>(A, task, smem, wave, lane STAMP_PASS);
}

#define ONE_LAUNCH 1
extern "C" void kernel_launch(void* const* d_in, const int* in_sizes, int n_in, void* d_out, int out_size, void* d_ws, size_t ws_size,
                              hipStream_t stream) {
  (void)in_sizes; (void)n_in; (void)out_size; (void)ws_size;
  Args A;
  A.x_img = (const char*)d_in[0]; A.x_txt = (const char*)d_in[1]; A.cvec = (const unsigned short*)d_in[2];
  A.wqkv_img = (const char*)d_in[3]; A.wqkv_txt = (const char*)d_in[4]; A.wo_img = (const char*)d_in[5]; A.wo_txt = (const char*)d_in[6];
  A.wmod_img = (const unsigned short*)d_in[7]; A.wmod_txt = (const unsigned short*)d_in[8];
  A.ws = (char*)d_ws; A.out = (char*)d_out;
  int gate = 0;
  for (int x = 0; x < 8; ++x) {
    const int sub = x & 3, qh = sub & 1, ch = sub >> 1;
    int img = 0, ntx = 0;
    for (int i = 0; i < 32; ++i) {
      unsigned t = 0; bool light = false;
      if (sub < 2) {
        if (i < 28) t = 1u | ((sub * 4 + (i & 3)) << 3) | ((i >> 2) << 7);
        else { light = true; const int sc = sub * 4 + i - 28; if (sc < 7) t = 3u | (8u << 3) | (sc << 7); }
      } else {
        const int v = (sub - 2) * 24 + i;
        if (i < 24 && v < 45) t = 2u | ((v / 5) << 3) | ((v % 5) << 7); else light = true;
      }
      if (light && t == 0 && gate < 12 * (x / 4 + 1)) { t = 4u | ((gate & 1) << 3) | ((gate >> 1) << 7); ++gate; }
      if (light && ntx < (sub < 2 ? 4 : 8)) { t |= (2u << 11) | ((sub < 2 ? sub * 4 + ntx : 8 + (sub - 2) * 8 + ntx) << 16); ++ntx; }
      else if (!light && img < 24) { t |= (1u << 11) | ((qh * 4 + (img & 3)) << 13) | ((ch * 6 + (img >> 2)) << 16); ++img; }
      else if (light && img < 24 && sub == 3) { t |= (1u << 11) | ((qh * 4 + (img & 3)) << 13) | ((ch * 6 + (img >> 2)) << 16); ++img; }
      A.task[i * 8 + x] = t;
    }
  }
  k0_prep<<<K0_GRID, 512, 0, stream>>>((const unsigned short*)A.x_img, (const unsigned short*)A.x_txt, A.cvec, A.wmod_img, A.wmod_txt, A.wqkv_img, A.wqkv_txt, A.ws);
  (void)hipFuncSetAttribute((const void*)k_main<7>, hipFuncAttributeMaxDynamicSharedMemorySize, LDS_MAIN);
  k_main<7><<<256, 768, LDS_MAIN, stream>>>(A);
}
```

```cpp
#include <hip/hip_runtime.h>
#include <hip/hip_bf16.h>
#include <stdint.h>

typedef short    bf16x8 __attribute__((ext_vector_type(8)));
typedef float    f32x16 __attribute__((ext_vector_type(16)));
typedef float    f32x4  __attribute__((ext_vector_type(4)));
typedef float    f32x2  __attribute__((ext_vector_type(2)));
typedef unsigned u32x4  __attribute__((ext_vector_type(4)));
typedef unsigned u32x2  __attribute__((ext_vector_type(2)));
typedef __bf16   bf16x2_t __attribute__((ext_vector_type(2)));

#define DI __device__ __forceinline__
#define LAS __attribute__((address_space(3)))
#define GAS __attribute__((address_space(1)))
#define SBAR() __builtin_amdgcn_sched_barrier(0)

constexpr int D = 768, NI = 1024, NTX = 32;
constexpr int TPB = 33;
constexpr int NTILE = 66;
constexpr int KS = 48;
#define PAD1 0
#define PAD2 0
constexpr int TILE_B = KS * 1024 + PAD1;
constexpr int KSK = 66;
constexpr int PT_B = KSK * 1024 + PAD2;
constexpr int NCH = 7;
constexpr int MSPLIT = 4;

constexpr size_t WS_FLAG = 0;
constexpr int F_QKV = 0, F_P = 51, F_VP = 69, F_GATE = 79, F_TXT = 103, F_N = 128;
constexpr size_t WS_MOD  = 32768;
constexpr size_t WS_XN   = WS_MOD + (size_t)2 * MSPLIT * 2 * 2304 * 4;
constexpr size_t WS_QKV  = WS_XN + (size_t)NTILE * TILE_B;
constexpr size_t WS_P    = WS_QKV + (size_t)3 * NTILE * TILE_B;
constexpr size_t WS_VP   = WS_P + (size_t)NTILE * PT_B;
constexpr size_t WS_ST   = WS_VP + (size_t)48 * PT_B;
constexpr size_t WS_AT   = WS_ST + (size_t)NTILE * NCH * 2 * 32 * 4;
constexpr size_t WS_END  = WS_AT + (size_t)2 * TILE_B;

constexpr float SM_C = 0.03608439182435161f * 1.4426950408889634f;

template <class T> DI T sel(bool c, T a, T b) { return c ? a : b; }
DI int imin0(int a, int b) { return a < b ? a : b; }
template <int I> struct IC { static constexpr int v = I; };
template <int B, int E, class F> DI void sfor(F&& f) { if constexpr (B < E) { f(IC<B>{}); sfor<B + 1, E>(f); } }

DI unsigned pk_bf16(float a, float b) { f32x2 v = {a, b}; bf16x2_t r = __builtin_convertvector(v, bf16x2_t); return __builtin_bit_cast(unsigned, r); }
DI float bf_lo(unsigned u) { return __uint_as_float(u << 16); }
DI float bf_hi(unsigned u) { return __uint_as_float(u & 0xffff0000u); }
DI f32x16 mfma(u32x4 a, u32x4 b, f32x16 c) {
  return __builtin_amdgcn_mfma_f32_32x32x16_bf16(__builtin_bit_cast(bf16x8, a), __builtin_bit_cast(bf16x8, b), c, 0, 0, 0);
}
template <bool FIRST> DI void mfma_v(f32x16& c, u32x4 a, u32x4 b) {
  if constexpr (FIRST) asm volatile("v_mfma_f32_32x32x16_bf16 %0, %1, %2, 0" : "=&v"(c) : "v"(a), "v"(b));
  else asm volatile("v_mfma_f32_32x32x16_bf16 %0, %1, %2, %0" : "+v"(c) : "v"(a), "v"(b));
}
DI float xhalf_max(float v) { auto r = __builtin_amdgcn_permlane32_swap(__float_as_uint(v), __float_as_uint(v), false, false); return fmaxf(__uint_as_float(r[0]), __uint_as_float(r[1])); }
DI float xhalf_sum(float v) { auto r = __builtin_amdgcn_permlane32_swap(__float_as_uint(v), __float_as_uint(v), false, false); return __uint_as_float(r[0]) + __uint_as_float(r[1]); }
DI u32x4 pack8(const f32x16& a, int s) {
  u32x4 r = {pk_bf16(a[8 * s], a[8 * s + 1]), pk_bf16(a[8 * s + 2], a[8 * s + 3]), pk_bf16(a[8 * s + 4], a[8 * s + 5]), pk_bf16(a[8 * s + 6], a[8 * s + 7])};
  return r;
}
DI unsigned xn_pack(float a, float b) {
  const unsigned lo = pk_bf16(a, 0.f);
  const unsigned m = __float_as_uint(fmaxf(fabsf(b), 1.17549435e-38f));
  const unsigned d = ((m + 0x8000u - lo) & 0x7fff0000u) | lo;
  return d | (__float_as_uint(b) & 0x80000000u);
}
DI unsigned mod_pair(unsigned x, float s0, float t0, float s1, float t1) {
  unsigned o, a, b;
  asm volatile("v_lshlrev_b32 %1, 16, %3\n\tv_fma_f32 %2, %3, %6, %7\n\tv_fma_f32 %1, %1, %4, %5\n\tv_cvt_pk_bf16_f32 %0, %1, %2"
      : "=v"(o), "=&v"(a), "=&v"(b) : "v"(x), "v"(s0), "v"(t0), "v"(s1), "v"(t1));
  return o;
}
DI const char* sadd(const char* p, unsigned d) {
  const unsigned long long u = (unsigned long long)p;
  unsigned lo, hi;
  asm("s_add_u32 %0, %2, %4\n\ts_addc_u32 %1, %3, 0" : "=&s"(lo), "=&s"(hi) : "s"((unsigned)u), "s"((unsigned)(u >> 32)), "s"(d) : "scc");
  return (const char*)(((unsigned long long)hi << 32) | lo);
}
DI const char* uni(const char* p) {
  unsigned long long u = (unsigned long long)p;
  unsigned lo = __builtin_amdgcn_readfirstlane((unsigned)u), hi = __builtin_amdgcn_readfirstlane((unsigned)(u >> 32));
  return (const char*)(((unsigned long long)hi << 32) | lo);
}

#define ST_POL "sc1"
DI void gst16(char* p, u32x4 v) { asm volatile("global_store_dwordx4 %0, %1, off " ST_POL "\n\ts_nop 1" ::"v"(p), "v"(v) : "memory"); }
#define WAIT_VM(n) asm volatile("s_waitcnt vmcnt(%0)" ::"n"(n) : "memory")
#define WAIT_LG0() asm volatile("s_waitcnt lgkmcnt(0)" ::: "memory")
template <int OFF> DI void gld16(u32x4& d, unsigned voff, const char* sb) {
  asm volatile("global_load_dwordx4 %0, %1, %2 offset:%3" : "=v"(d) : "v"(voff), "s"(sb), "i"(OFF) : "memory");
}
template <int OFF> DI void gld16a(u32x4& d, unsigned voff, const char* sb) {
  asm volatile("global_load_dwordx4 %0, %1, %2 offset:%3" : "=a"(d) : "v"(voff), "s"(sb), "i"(OFF) : "memory");
}
template <int OFF> DI void gld8(u32x2& d, unsigned voff, const char* sb) {
  asm volatile("global_load_dwordx2 %0, %1, %2 offset:%3" : "=v"(d) : "v"(voff), "s"(sb), "i"(OFF) : "memory");
}
template <int OFF> DI void gld4(float& d, unsigned voff, const char* sb) {
  asm volatile("global_load_dword %0, %1, %2 offset:%3 sc1" : "=v"(d) : "v"(voff), "s"(sb), "i"(OFF) : "memory");
}
template <int OFF> DI void touch16(u32x4& junk, unsigned voff, const char* sb) {
  asm volatile("global_load_dwordx4 %0, %1, %2 offset:%3" : "+v"(junk) : "v"(voff), "s"(sb), "i"(OFF) : "memory");
}
DI void touch(unsigned& junk, unsigned voff, const char* sb) {
  asm volatile("global_load_dword %0, %1, %2" : "+v"(junk) : "v"(voff), "s"(sb) : "memory");
}
template <int OFF> DI void gld16_sc1(u32x4& d, unsigned voff, const char* sb) {
  asm volatile("global_load_dwordx4 %0, %1, %2 offset:%3 sc1" : "=v"(d) : "v"(voff), "s"(sb), "i"(OFF) : "memory");
}
#define GLDS5(NAME, POL) DI void NAME(const unsigned (&v)[5], const char* sb, unsigned lds) { \
  asm volatile("s_mov_b32 m0, %6\n\ts_nop 0\n\t" \
      "global_load_lds_dwordx4 %0, %5 " POL "\n\t" \
      "global_load_lds_dwordx4 %1, %5 offset:1024 " POL "\n\t" \
      "global_load_lds_dwordx4 %2, %5 offset:2048 " POL "\n\t" \
      "global_load_lds_dwordx4 %3, %5 offset:3072 " POL "\n\t" \
      "s_add_u32 m0, m0, 4096\n\ts_nop 0\n\t" \
      "global_load_lds_dwordx4 %4, %5 " POL \
      ::"v"(v[0]), "v"(v[1]), "v"(v[2]), "v"(v[3]), "v"(v[4]), "s"(sb), "s"(lds) : "memory", "scc"); }
#define W_POL ""
GLDS5(glds5, "sc1")
GLDS5(glds5w, W_POL)
template <int N> DI void gldsN(unsigned v, const char* sb, unsigned lds) {
  static_assert(N >= 1 && N <= 4, "");
  if constexpr (N == 1) asm volatile("s_mov_b32 m0, %2\n\ts_nop 0\n\tglobal_load_lds_dwordx4 %0, %1 sc1" ::"v"(v), "s"(sb), "s"(lds) : "memory");
  if constexpr (N == 2) asm volatile("s_mov_b32 m0, %2\n\ts_nop 0\n\tglobal_load_lds_dwordx4 %0, %1 sc1\n\tglobal_load_lds_dwordx4 %0, %1 offset:1024 sc1" ::"v"(v), "s"(sb), "s"(lds) : "memory");
  if constexpr (N == 3) asm volatile("s_mov_b32 m0, %2\n\ts_nop 0\n\tglobal_load_lds_dwordx4 %0, %1 sc1\n\tglobal_load_lds_dwordx4 %0, %1 offset:1024 sc1\n\tglobal_load_lds_dwordx4 %0, %1 offset:2048 sc1" ::"v"(v), "s"(sb), "s"(lds) : "memory");
  if constexpr (N == 4) asm volatile("s_mov_b32 m0, %2\n\ts_nop 0\n\tglobal_load_lds_dwordx4 %0, %1 sc1\n\tglobal_load_lds_dwordx4 %0, %1 offset:1024 sc1\n\tglobal_load_lds_dwordx4 %0, %1 offset:2048 sc1\n\tglobal_load_lds_dwordx4 %0, %1 offset:3072 sc1" ::"v"(v), "s"(sb), "s"(lds) : "memory");
}
template <int OFF> DI void lds_tr(u32x2& d, unsigned a) { asm volatile("ds_read_b64_tr_b16 %0, %1 offset:%2" : "=v"(d) : "v"(a), "i"(OFF) : "memory"); }
template <int O0, int O1> DI void lds_tr2(u32x4& d, unsigned a) {
  u32x2 lo, hi;
  asm volatile("ds_read_b64_tr_b16 %0, %2 offset:%3\n\tds_read_b64_tr_b16 %1, %2 offset:%4" : "=&v"(lo), "=&v"(hi) : "v"(a), "i"(O0), "i"(O1) : "memory");
  d = u32x4{lo[0], lo[1], hi[0], hi[1]};
}
template <int OFF> DI void lds_128(u32x4& d, unsigned a) { asm volatile("ds_read_b128 %0, %1 offset:%2" : "=v"(d) : "v"(a), "i"(OFF) : "memory"); }
template <int OFF> DI void lds_128f(f32x4& d, unsigned a) { asm volatile("ds_read_b128 %0, %1 offset:%2" : "=v"(d) : "v"(a), "i"(OFF) : "memory"); }
template <class T> DI void tie1(T& x) { asm volatile("" : "+v"(x)); }
template <class... T> DI void tie(T&... x) { (tie1(x), ...); }
template <class T> DI void tie1a(T& x) { asm volatile("" : "+a"(x)); }
template <class... T> DI void tiea(T&... x) { (tie1a(x), ...); }

constexpr int K0_MODWG = 2 * 24 * MSPLIT;
constexpr int K0_LNWG = 2 * NTILE;
constexpr int CODE_KB = 56;
constexpr int K0_PFWG = 2;
constexpr int K0_GRID = K0_LNWG + K0_MODWG / 2 + K0_PFWG;
template <int NT, int ROWS, bool DEV, bool PRE>
DI void mod_slab(const unsigned short* __restrict__ W, const unsigned short* __restrict__ cvec, int row0, int col0, float* __restrict__ out, float* sm, int tid,
                 u32x4 (&w)[ROWS * 8 / NT]  , unsigned c0, unsigned c1) {
  constexpr int RG = NT / 8, NLD = ROWS / RG;
  static_assert(RG % 16 == 0 && ROWS % RG == 0 && ROWS / 2 <= NT, "");
  const int cg = tid & 7, rg = tid >> 3;
  if constexpr (!PRE) {
    if (tid < ROWS / 2) { c0 = *(const unsigned*)(cvec + row0 + 2 * tid); c1 = *(const unsigned*)(cvec + D + row0 + 2 * tid); }
    const unsigned short* wp = W + (size_t)(row0 + rg) * (6 * D) + col0 + cg * 8;
#pragma unroll
    for (int i = 0; i < NLD; ++i) w[i] = __builtin_nontemporal_load((const u32x4*)(wp + (size_t)i * RG * (6 * D)));
  }
  float* sc = sm + RG * 128 + 64;
  if (tid < ROWS / 2) {
    auto silu = [](float v) { return v / (1.f + __expf(-v)); };
    const unsigned s0 = pk_bf16(silu(bf_lo(c0)), silu(bf_hi(c0))), s1 = pk_bf16(silu(bf_lo(c1)), silu(bf_hi(c1)));
    *(f32x2*)(sc + 2 * tid) = f32x2{bf_lo(s0), bf_hi(s0)};
    *(f32x2*)(sc + ROWS + 2 * tid) = f32x2{bf_lo(s1), bf_hi(s1)};
  }
  __syncthreads();
  float a0[8], a1[8];
#pragma unroll
  for (int j = 0; j < 8; ++j) { a0[j] = 0.f; a1[j] = 0.f; }
#pragma unroll
  for (int i = 0; i < NLD; ++i) {
    const float s0 = sc[rg + RG * i], s1 = sc[ROWS + rg + RG * i];
#pragma unroll
    for (int j = 0; j < 4; ++j) {
      const float lo = bf_lo(w[i][j]), hi = bf_hi(w[i][j]);
      a0[2 * j] = fmaf(s0, lo, a0[2 * j]); a0[2 * j + 1] = fmaf(s0, hi, a0[2 * j + 1]);
      a1[2 * j] = fmaf(s1, lo, a1[2 * j]); a1[2 * j + 1] = fmaf(s1, hi, a1[2 * j + 1]);
    }
  }
  f32x4* rp = (f32x4*)(sm + rg * 128 + (rg >= RG / 2 ? 16 : 0) + cg * 8);
  rp[0] = f32x4{a0[0], a0[1], a0[2], a0[3]}; rp[1] = f32x4{a0[4], a0[5], a0[6], a0[7]};
  rp[16] = f32x4{a1[0], a1[1], a1[2], a1[3]}; rp[17] = f32x4{a1[4], a1[5], a1[6], a1[7]};
  __syncthreads();
  if (tid < 256) {
    const int o = tid >> 1, hf = tid & 1;
    float s = 0.f;
#pragma unroll
    for (int r = 0; r < RG / 2; ++r) s += sm[(hf * (RG / 2) + r) * 128 + hf * 16 + o];
    s += __shfl_xor(s, 1);
    if (!hf) {
      if constexpr (DEV) __hip_atomic_store((GAS float*)out + (o >> 6) * 2304 + (o & 63), s, __ATOMIC_RELAXED, __HIP_MEMORY_SCOPE_AGENT);
      else out[(o >> 6) * 2304 + (o & 63)] = s;
    }
  }
}
DI float x16_sum(float v) { auto r = __builtin_amdgcn_permlane16_swap(__float_as_uint(v), __float_as_uint(v), false, false); return __uint_as_float(r[0]) + __uint_as_float(r[1]); }
__global__ __launch_bounds__(512) void k0_prep(const unsigned short* __restrict__ x_img, const unsigned short* __restrict__ x_txt,
                                               const unsigned short* __restrict__ cvec, const unsigned short* __restrict__ wmod_img,
                                               const unsigned short* __restrict__ wmod_txt, const char* __restrict__ wqkv_img,
                                               const char* __restrict__ wqkv_txt, char* __restrict__ ws) {
  __shared__ __attribute__((aligned(16))) float sm2[2][4096 + 64 + 384];
  const int bid = blockIdx.x;
  const bool is_ln = bid < K0_LNWG, is_mod = !is_ln & (bid < K0_LNWG + K0_MODWG / 2);
  const int tt = bid >> 1, half = bid & 1, b = tt >= TPB, i = tt - b * TPB;
  const int w = threadIdx.x >> 6, lane = threadIdx.x & 63, r = lane & 15, h = (lane >> 4) & 1, kk = lane >> 5;
  const int m = (bid - K0_LNWG) * 2 + (threadIdx.x >> 8), ksp = m & 3, type = (m >> 2) & 1, slab = m >> 3;
  const char* xa = sel(i < 32, (const char*)x_img + (unsigned)((b * NI + i * 32 + half * 16 + r) * D * 2), (const char*)x_txt + (unsigned)((b * NTX + half * 16 + r) * D * 2)) + (6 * w + kk) * 32 + 16 * h;
  const char* ma = (const char*)sel((bool)type, wmod_txt, wmod_img) + (unsigned)(((ksp * (D / MSPLIT) + ((threadIdx.x & 255) >> 3)) * (6 * D) + slab * 64 + (threadIdx.x & 7) * 8) * 2);
  const char* base0 = sel(is_mod, ma, sel(is_ln, xa, (const char*)x_img));
  const unsigned st0 = sel(is_mod, 32 * 6 * D * 2, 64);
  const int ci = ksp * (D / MSPLIT) + 2 * imin0(threadIdx.x & 255, D / MSPLIT / 2 - 1);
  const unsigned c0 = *(const GAS unsigned*)(cvec + ci), c1 = *(const GAS unsigned*)(cvec + D + ci);
  u32x4 v[6];
#pragma unroll
  for (int s = 0; s < 3; ++s) v[s] = __builtin_nontemporal_load((const GAS u32x4*)(base0 + s * st0));
  if (is_mod) {
#pragma unroll
    for (int s = 3; s < 6; ++s) v[s] = __builtin_nontemporal_load((const GAS u32x4*)(base0 + s * st0));
  }
  if (is_ln) {
    float* sm = sm2[0];
    float sum = 0.f;
#pragma unroll
    for (int s = 0; s < 3; ++s)
#pragma unroll
      for (int j = 0; j < 4; ++j) sum += bf_lo(v[s][j]) + bf_hi(v[s][j]);
    const float mw = xhalf_sum(x16_sum(sum)) * (1.f / 96.f);
    float sq = 0.f;
#pragma unroll
    for (int s = 0; s < 3; ++s)
#pragma unroll
      for (int j = 0; j < 4; ++j) { const float a = bf_lo(v[s][j]) - mw, c = bf_hi(v[s][j]) - mw; sq = fmaf(a, a, sq); sq = fmaf(c, c, sq); }
    sq = xhalf_sum(x16_sum(sq));
    if (lane < 16) *(f32x2*)(sm + (r * 8 + w) * 2) = f32x2{mw, sq};
    __syncthreads();
    f32x4 p[4];
#pragma unroll
    for (int q = 0; q < 4; ++q) p[q] = *(const f32x4*)(sm + r * 16 + q * 4);
    const float mean = ((p[0][0] + p[0][2]) + (p[1][0] + p[1][2]) + (p[2][0] + p[2][2]) + (p[3][0] + p[3][2])) * 0.125f;
    float m2 = 0.f, dd = 0.f;
#pragma unroll
    for (int q = 0; q < 4; ++q) { const float d0 = p[q][0] - mean, d1 = p[q][2] - mean; m2 += p[q][1] + p[q][3]; dd = fmaf(d0, d0, fmaf(d1, d1, dd)); }
    const float rstd = 1.f / sqrtf((m2 + 96.f * dd) * (1.f / D) + 1e-5f);
    char* dst = ws + WS_XN + (size_t)tt * TILE_B + (size_t)(6 * w + kk) * 1024 + (h * 32 + half * 16 + r) * 16;
#pragma unroll
    for (int s = 0; s < 3; ++s) {
      u32x4 o;
#pragma unroll
      for (int j = 0; j < 4; ++j) o[j] = xn_pack((bf_lo(v[s][j]) - mean) * rstd, (bf_hi(v[s][j]) - mean) * rstd);
      gst16(dst + s * 2048, o);
    }
  } else if (is_mod) {
    mod_slab<256, D / MSPLIT, false, true>(wmod_img, cvec, ksp * (D / MSPLIT), slab * 64,
                              (float*)(ws + WS_MOD) + (size_t)(type * MSPLIT + ksp) * 2 * 2304 + slab * 64, sm2[threadIdx.x >> 8], threadIdx.x & 255, v, c0, c1);
  } else {
    const int pw = bid - (K0_LNWG + K0_MODWG / 2);
    if (pw == 0 && threadIdx.x < F_N) *(unsigned*)(ws + WS_FLAG + threadIdx.x * 256) = 0u;
    if (pw == 1) {
      unsigned lo, hi;
      asm volatile("s_getpc_b64 s[20:21]\n\ts_add_u32 %0, s20, _Z6k_mainILi7EEv4Args@rel32@lo+4\n\ts_addc_u32 %1, s21, _Z6k_mainILi7EEv4Args@rel32@hi+12"
                   : "=s"(lo), "=s"(hi)::"s20", "s21", "scc");
      const char* code = (const char*)(((unsigned long long)hi << 32) | lo) + threadIdx.x * 16;
      u32x4 c[CODE_KB / 8];
#pragma unroll
      for (int i = 0; i < CODE_KB / 8; ++i) c[i] = *(const GAS u32x4*)(code + i * 8192);
#pragma unroll
      for (int i = 0; i < CODE_KB / 8; ++i) asm volatile("" ::"v"(c[i]));
    }
    asm volatile("" ::"v"(v[0]), "v"(v[1]), "v"(v[2]));
  }
}

#define BAR() __builtin_amdgcn_s_barrier()
#define WAIT_LG(n) asm volatile("s_waitcnt lgkmcnt(%0)" ::"n"(n) : "memory")
constexpr int imin(int a, int b) { return a < b ? a : b; }
constexpr int imax(int a, int b) { return a > b ? a : b; }
constexpr int NSTG = 24;
constexpr int LDS_MAIN = 163840;
constexpr int ld_cnt(int k, int ns, int n) { return k == 0 ? 3 : imin(imin(imin(n, k + 12), ld_cnt(k - 1, ns, n) + 2), imax(ns, k - 1 + ns - 1)); }
DI void bars(int n) {
#pragma unroll 1
  for (int i = 0; i < n; ++i) BAR();
}
template <int N, int NS, int PER, bool PRE, class F> DI void ring_loader(F&& issue) {
  sfor<0, N>([&](auto K) {
    constexpr int k = decltype(K)::v;
    sfor<(k == 0 ? 0 : ld_cnt(k - 1, NS, N)), ld_cnt(k, NS, N)>(issue);
    if constexpr (PRE && k == 0) BAR();
    WAIT_VM(PER * (ld_cnt(k, NS, N) - k - 1)); BAR();
  });
}
#define T_L20 6
#define T_L2S 4
constexpr int ld2_cnt(int k, int ns, int n) { return k == 0 ? T_L20 : imin(imin(n, ld2_cnt(k - 1, ns, n) + T_L2S), imax(ns, k - 1 + ns - 1)); }
template <int N, int NS, int PER, int PAR, class F> DI void ring_loader2(F&& issue, bool pre) {
  sfor<0, N>([&](auto K) {
    constexpr int k = decltype(K)::v, c = ld2_cnt(k, NS, N);
    sfor<(k == 0 ? 0 : ld2_cnt(k - 1, NS, N)), c>([&](auto G) { if constexpr ((decltype(G)::v & 1) == PAR) issue(G); });
    if constexpr (k == 0) { if (pre) BAR(); }
    WAIT_VM(PER * ((c + 1 - PAR) / 2 - (k + 2 - PAR) / 2)); BAR();
  });
}
DI void glds2x2(unsigned v, const char* sb0, const char* sb1m  , unsigned lds) {
  asm volatile(
      "s_mov_b32 m0, %3\n\ts_nop 0\n\t"
      "global_load_lds_dwordx4 %0, %1 sc1\n\t"
      "global_load_lds_dwordx4 %0, %1 offset:1024 sc1\n\t"
      "global_load_lds_dwordx4 %0, %2 offset:2048 sc1\n\t"
      "global_load_lds_dwordx4 %0, %2 offset:3072 sc1"
      ::"v"(v), "s"(sb0), "s"(sb1m), "s"(lds) : "memory");
}
DI void flag_add(char* ws, int f) { __hip_atomic_fetch_add((GAS unsigned*)(ws + WS_FLAG + f * 256), 1u, __ATOMIC_RELAXED, __HIP_MEMORY_SCOPE_AGENT); }
#define T_NAP 3
template <bool FUSED, int NAP = T_NAP> DI void flag_wait(char* ws, int f  , unsigned want  ) {
  if constexpr (FUSED) {
    GAS unsigned* p = (GAS unsigned*)(ws + WS_FLAG + f * 256);
#pragma unroll 1
    for (unsigned spins = 0; spins < (1u << 17); ++spins) {
      if (__all(__hip_atomic_load(p, __ATOMIC_RELAXED, __HIP_MEMORY_SCOPE_AGENT) >= want)) break;
      __builtin_amdgcn_s_sleep(NAP);
    }
    asm volatile("" ::: "memory");
  }
}

DI void w_voff(unsigned (&voff)[5], int lane, unsigned pitch) {
#pragma unroll
  for (int i = 0; i < 5; ++i) {
    const unsigned o = i * 64 + lane, row = o / 20, cb = (o % 20) * 16;
    voff[i] = row * pitch + cb - (i < 4 ? i * 1024 : 0);
  }
}

struct Args {
  const char *x_img, *x_txt, *wqkv_img, *wqkv_txt, *wo_img, *wo_txt;
  const unsigned short *cvec, *wmod_img, *wmod_txt;
  char *ws, *out;
  unsigned task[256];
};

#define T_HD 3
#define T_LA 7
#define T_XR 2
#define HD0 1
constexpr int W_SLOT = 10240, P1_NW = 10, P1_NH = 6, HD = T_HD, PF_START = 10;
constexpr int x_iss(int hj) { return hj < 0 ? 4 : imin(24, imin(4 + T_XR * (hj + 1), hj + 1 + 2 * HD)); }
constexpr int P1_H = P1_NW * W_SLOT;
constexpr int P1_MV = P1_H + P1_NH * 8192;
DI void phase1(const Args& A, const char* A_code, char* smem, int wave, int lane) {
  char* ws = A.ws;
  const int bid = blockIdx.x, xcd = bid & 7, idx = bid >> 3;
  const int rowhalf = xcd & 1, colq = xcd >> 1, h = lane >> 5;
  const bool txt = (colq == 3) & (idx >= 24);
  const int ct = sel(txt, rowhalf * 8 + (idx - 24), colq * 4 + (idx >> 3));
  const int n0 = sel(ct < 14, ct * 160, 2144);
  const unsigned lds0 = (unsigned)(size_t)(LAS char*)smem;
  const unsigned l16 = lane * 16;
  const int j = wave & 3;
  const char* xb = ws + WS_XN + (unsigned)(sel(txt, 32, rowhalf * TPB + (idx & 7) * 4) * TILE_B + j * 1024);
  const char* W = sel(txt, A.wqkv_txt, A.wqkv_img) + n0 * 2;
  const unsigned mr = imin(threadIdx.x, 383);
  const char* mp = ws + WS_MOD + (unsigned)(sel(txt, MSPLIT * 2, rowhalf) * 2304 * 4);
  asm volatile("s_nop 4" ::"s"(xb), "s"(W), "s"(mp) : "memory");
  if (ct >= 15) return;
  u32x4 m[MSPLIT];
  auto ldm = [&](const char* q) {
    const unsigned src = (mr < 192 ? 192 + mr : mr - 192) * 16;
    sfor<0, MSPLIT>([&](auto K) { constexpr int k = decltype(K)::v; gld16<0>(m[k], src, q + k * 18432); });
  };
  auto stm1 = [&](int bb) {
    tie(m[0], m[1], m[2], m[3]);
    auto f = [](u32x4 v) { return __builtin_bit_cast(f32x4, v); };
    *(f32x4*)(smem + P1_MV + bb * 6144 + mr * 16) = (f(m[0]) + f(m[1])) + (f(m[2]) + f(m[3])) + (mr < 192 ? 1.f : 0.f);
  };
  auto stm = [&]() {
    if (wave < 6) {
      stm1(0);
      if (txt) { ldm(mp + 2304 * 4); WAIT_VM(0); stm1(1); }
    }
    WAIT_LG(0);
    BAR();
  };
  if (__builtin_expect(wave >= 8, 1)) {
    if (wave >= 10) {
      u32x4 junk = {0, 0, 0, 0};
      const char* wo = sel((bool)(bid & 1), A.wo_txt, A.wo_img) + (bid >> 1) * 9216;
      const char* wg = (const char*)sel((bool)(bid & 1), A.wmod_txt, A.wmod_img) + (unsigned)((bid >> 1) * 6 * 9216 + 3072);
      const char* src = uni(sel(wave == 10, wo, wg));
      if (wave == 11 && idx < CODE_KB / 2) {
        const char* code = A_code + idx * 2048;
        touch16<0>(junk, l16, code); touch16<1024>(junk, l16, code);
      }
      bars(PF_START);
#pragma unroll 1
      for (int i = 0; i < 9; ++i) {
        const unsigned o = i * 64 + lane, row = o / 96;
        touch16<0>(junk, sel(wave == 10, i * 1024 + l16, row * 9216 + (o - row * 96) * 16), src);
        BAR();
      }
      bars(NSTG + 1 - 9 - PF_START);
      WAIT_VM(0); tie(junk);
    } else {
      unsigned voff[5];
      w_voff(voff, lane, 4608);
      const char* p = W + (wave - 8) * 16 * 4608;
      const unsigned lds = lds0 + (wave - 8) * 5120;
      ring_loader<NSTG, P1_NW, 5, true>([&](auto G) { glds5w(voff, p, lds + (decltype(G)::v % P1_NW) * W_SLOT); p += 32 * 4608; });
    }
  } else if (wave >= 4) {
    const unsigned x1 = l16 + sel(txt, TPB, 1) * TILE_B, x2 = l16 + sel(txt, 0, 2) * TILE_B, x3 = l16 + sel(txt, TPB, 3) * TILE_B;
    u32x4 xr[12][4];
    auto ldx = [&](auto I, auto U) {
      constexpr int i = decltype(I)::v, u = decltype(U)::v;
      if constexpr (i < 12) { gld16<0>(xr[i][u], u ? x1 : l16, xb + i * 4096); gld16<0>(xr[i][u + 2], u ? x3 : x2, xb + i * 4096); }
    };
    if (wave < 6) ldm(mp);
    sfor<0, HD0>([&](auto I) { ldx(I, IC<0>{}); ldx(I, IC<1>{}); });
    WAIT_VM(4 * HD0);
    stm();
    ldx(IC<1>{}, IC<0>{}); ldx(IC<1>{}, IC<1>{});
    auto more_x = [&](auto HJ) {
      constexpr int hj = decltype(HJ)::v;
      sfor<x_iss(hj - 1), x_iss(hj)>([&](auto U) { constexpr int u = decltype(U)::v; ldx(IC<u / 2>{}, IC<u % 2>{}); });
    };
    const unsigned mva0 = lds0 + P1_MV + h * 32 + j * 64, mva1 = mva0 + sel(txt, 6144, 0);
    char* hw = smem + P1_H + ((j >> 1) * 2 + (j & 1)) * 4096 + l16;
    f32x4 mv[2][4];
    auto rdm = [&](auto I, auto U) {
      constexpr int i = decltype(I)::v, u = decltype(U)::v;
      const unsigned a = u ? mva1 : mva0;
      if constexpr (i < 12) { lds_128f<i * 256>(mv[u][0], a); lds_128f<i * 256 + 16>(mv[u][1], a); lds_128f<3072 + i * 256>(mv[u][2], a); lds_128f<3072 + i * 256 + 16>(mv[u][3], a); }
    };
    auto modw = [&](auto I, auto T) {
      constexpr int i = decltype(I)::v, t = decltype(T)::v, q = t & 1;
      const u32x4 x = xr[i][t];
      u32x4 o;
#pragma unroll
      for (int e = 0; e < 4; ++e)
        o[e] = mod_pair(x[e], mv[q][e >> 1][(e & 1) * 2], mv[q][2 + (e >> 1)][(e & 1) * 2], mv[q][e >> 1][(e & 1) * 2 + 1], mv[q][2 + (e >> 1)][(e & 1) * 2 + 1]);
      *(u32x4*)(hw + ((2 * i) % P1_NH) * 8192 + t * 1024) = o;
    };
    rdm(IC<0>{}, IC<0>{}); rdm(IC<0>{}, IC<1>{});
    sfor<0, 12>([&](auto I) {
      constexpr int i = decltype(I)::v, mvn = i < 11 ? 4 : 0;
      WAIT_VM(2 * (x_iss(2 * i - 1) - 2 * i - 1)); WAIT_LG(i ? 6 : 4);
      modw(I, IC<0>{}); modw(I, IC<2>{});
      if constexpr (i > 0) more_x(IC<2 * i>{});
      rdm(IC<i + 1>{}, IC<0>{});
      WAIT_LG(2 + mvn);
      if constexpr (i > 0) { if (i > 1 || j >= 2) BAR(); }
      WAIT_VM(2 * (x_iss(i ? 2 * i : -1) - 2 * i - 2));
      modw(I, IC<1>{}); modw(I, IC<3>{});
      if constexpr (i > 0) more_x(IC<2 * i + 1>{});
      if constexpr (i == 0) WAIT_LG(0);
      rdm(IC<i + 1>{}, IC<1>{});
      WAIT_LG(2 + mvn);
      BAR();
      if constexpr (i == 0) { more_x(IC<0>{}); more_x(IC<1>{}); }
    });
    WAIT_LG(0);
    BAR();
    if (j < 2) BAR();
  } else {
    ldm(mp); WAIT_VM(0); stm();
    f32x16 acc[5];
    const int blk = (lane >> 4) & 1, q4 = (lane & 15) >> 2, p = lane & 3;
    unsigned rw = lds0 + (8 * h + q4) * 320 + blk * 32 + p * 8, rw1 = rw + 5 * W_SLOT;
    unsigned rh = lds0 + P1_H + wave * 1024 + l16;
    tie(rw, rw1, rh);
    constexpr int LA = T_LA;
    u32x4 wf[5 * KS], hh[KS];
    auto rdw = [&](auto N) {
      constexpr int n = decltype(N)::v, s = n / 5, c = n % 5, slot = (s >> 1) % P1_NW, so = (slot % 5) * W_SLOT + (s & 1) * 5120;
      if constexpr (s < KS) {
        if constexpr (c == 0 && (s & 1) == 0 && s > 0) BAR();
        if constexpr (c == 0) lds_128<((s >> 1) % P1_NH) * 8192 + (s & 1) * 4096>(hh[s], rh);
        lds_tr2<so + c * 64, so + c * 64 + 1280>(wf[n], slot < 5 ? rw : rw1);
      }
    };
    BAR();
    sfor<0, LA>(rdw);
    sfor<0, 5 * KS>([&](auto N) {
      constexpr int n = decltype(N)::v, s = n / 5, c = n % 5;
      constexpr int hi = imin(n + LA - 1, 5 * KS - 1);
      WAIT_LG(2 * (hi - n) + (hi / 5 - n / 5));
      mfma_v<s == 0>(acc[c], wf[n], hh[s]);
      rdw(IC<n + LA>{});
    });
    asm volatile("s_nop 15" : "+v"(acc[0]), "+v"(acc[1]), "+v"(acc[2]), "+v"(acc[3]), "+v"(acc[4]));
    const int tt = sel(txt, (wave & 1) * TPB + 32, rowhalf * TPB + (idx & 7) * 4 + wave);
    if (!(txt && wave >= 2)) {
#pragma unroll
      for (int c = 0; c < 5; ++c) {
        const int f0 = n0 + c * 32;
        if (ct == 14 && c < 3) continue;
        const int mat = f0 / D, st = (f0 % D) >> 4;
        char* dst = ws + WS_QKV + ((size_t)mat * NTILE + tt) * TILE_B + st * 1024 + l16;
        gst16(dst, pack8(acc[c], 0));
        gst16(dst + 1024, pack8(acc[c], 1));
      }
    }
    WAIT_VM(0);
  }
  BAR();
  if (threadIdx.x == 0) {
    const int rb = sel(txt, 16, rowhalf * 8 + (idx & 7)), m0 = (ct * 160) / D, m1 = imin(ct * 160 + 159, 2303) / D;
    flag_add(ws, F_QKV + rb * 3 + m0);
    if (m1 != m0) flag_add(ws, F_QKV + rb * 3 + m1);
  }
}

#define T_RA2 10
constexpr int P2_NS = 14, P2_RA = T_RA2;
template <int MODE>
DI void p2_consumer(f32x16 (&acc)[5], const char* priv, unsigned lds0, int lane) {
  const int h = lane >> 5, blk = (lane >> 4) & 1, q4 = (lane & 15) >> 2, p = lane & 3;
  const unsigned l16 = lane * 16;
  constexpr int PW = MODE == 2 ? 1 : 2;
  u32x4 xr[KS], wf[2][5];
  auto ldx = [&](auto S) { constexpr int s = decltype(S)::v; if constexpr (s < KS) gld16_sc1<(s & 3) * 1024>(xr[s], l16, priv + (s >> 2) * 4096); };
  sfor<0, P2_RA>(ldx);
  unsigned rw = MODE == 1 ? lds0 + (4 * h + q4) * 320 + blk * 32 + p * 8 : lds0 + l16, rw1 = rw + 5 * W_SLOT, rw2 = rw + 10 * W_SLOT;
  tie(rw, rw1, rw2);
  auto rdw = [&](auto S, auto C) {
    constexpr int s = decltype(S)::v, c = decltype(C)::v, cur = s & 1, slot = (s >> 1) % P2_NS, so = (slot % 5) * W_SLOT + (s & 1) * 5120;
    if constexpr (s < KS) {
      const unsigned a = slot < 5 ? rw : (slot < 10 ? rw1 : rw2);
      if constexpr (MODE == 2) lds_128<so + c * 1024>(wf[cur][c], a);
      else lds_tr2<so + c * 64, so + c * 64 + 2560>(wf[cur][c], a);
    }
  };
  BAR();
  sfor<0, 5>([&](auto C) { rdw(IC<0>{}, C); });
  sfor<0, KS>([&](auto S) {
    constexpr int s = decltype(S)::v, cur = s & 1;
    constexpr bool more = s + 1 < KS;
    if constexpr ((s & 1) && more) BAR();
    WAIT_VM(imin(P2_RA - 1, KS - 1 - s));
    sfor<0, 5>([&](auto C) {
      constexpr int c = decltype(C)::v;
      WAIT_LG((4 - c) * PW + (more ? c * PW : 0));
      if constexpr (MODE == 1) mfma_v<s == 0>(acc[c], xr[s], wf[cur][c]);
      else mfma_v<s == 0>(acc[c], wf[cur][c], xr[s]);
      rdw(IC<s + 1>{}, C);
      if constexpr (c == 2) ldx(IC<s + P2_RA>{});
    });
  });
  asm volatile("s_nop 15" : "+v"(acc[0]), "+v"(acc[1]), "+v"(acc[2]), "+v"(acc[3]), "+v"(acc[4]));
}
template <int NT> DI void p2_softmax(f32x16 (&acc)[5], char* dst  , float* st, int own0, int lane) {
  float m = -3.0e38f;
#pragma unroll
  for (int c = 0; c < NT; ++c)
    if (c >= own0) {
#pragma unroll
      for (int r = 0; r < 16; ++r) m = fmaxf(m, acc[c][r]);
    }
  m = xhalf_max(m);
  const float mc = -m * SM_C;
  float l = 0.f;
#pragma unroll
  for (int c = 0; c < NT; ++c)
    if (c >= own0) {
#pragma unroll
      for (int r = 0; r < 16; ++r) { const float pv = __builtin_amdgcn_exp2f(fmaf(acc[c][r], SM_C, mc)); acc[c][r] = pv; l += pv; }
      gst16(dst + c * 2048, pack8(acc[c], 0));
      gst16(dst + c * 2048 + 1024, pack8(acc[c], 1));
    }
  l = xhalf_sum(l);
  __hip_atomic_store((GAS float*)st + lane, lane < 32 ? m : l, __ATOMIC_RELAXED, __HIP_MEMORY_SCOPE_AGENT);
}
template <bool FUSED> DI void phase2(const Args& A, unsigned task, char* smem, int wave, int lane) {
  char* ws = A.ws;
  const int b = (blockIdx.x >> 2) & 1, kind = task & 7;
  const int pb = (task >> 3) & 15;
  const int sc = (task >> 7) & 15;
  const unsigned lds0 = (unsigned)(size_t)(LAS char*)smem;
  const unsigned l16 = lane * 16;
  const bool isS = kind == 1;
  const int kt0 = sel(sc < 6, sc * 5, 28);
  const int n0 = sel(sc < 4, sc * 160, 608);
  if (kind == 0) return;
  if (kind == 4) {
    u32x4 w[8];
    mod_slab<768, D, true, false>(pb ? A.wmod_txt : A.wmod_img, A.cvec, 0, 1536 + sc * 64, (float*)(ws + WS_MOD) + (size_t)pb * MSPLIT * 2 * 2304 + 1536 + sc * 64, (float*)smem, threadIdx.x, w, 0u, 0u);
    WAIT_VM(0);
    BAR();
    if (threadIdx.x == 0) flag_add(ws, F_GATE + pb * 12 + sc);
    return;
  }
  if (kind == 3) {
    const int qt = b * TPB + 32, own0 = sc < 6 ? 0 : 2;
    float* xm = (float*)(smem + KS * 1024);
    if (wave == 5) flag_wait<FUSED>(ws, sel(lane & 2, F_QKV + 16 * 3, F_QKV + 1 + 3 * sel(lane & 1, sc < 6 ? b * 8 + ((kt0 + 4) >> 2) : 16, b * 8 + (kt0 >> 2))), sel(lane & 2, 5u, 6u));
    BAR();
    if (wave >= 8) {
      const char* q = uni(ws + WS_QKV + (unsigned)(qt * TILE_B + (wave - 8) * 12288));
      asm volatile("s_nop 4" ::"s"(q) : "memory");
      sfor<0, 3>([&](auto I) { constexpr int i = decltype(I)::v; gldsN<4>(l16, q + i * 4096, lds0 + (wave - 8) * 12288 + i * 4096); });
      WAIT_VM(0);
      bars(3);
    } else if (wave >= 5) bars(3);
    else {
      const int kt = kt0 + wave;
      const char* kp = uni(ws + WS_QKV + (unsigned)((NTILE + b * TPB + kt) * TILE_B));
      asm volatile("s_nop 4" ::"s"(kp) : "memory");
      constexpr int RA = 24;
      u32x4 kf[KS], qf[KS];
      auto ldk = [&](auto S) { constexpr int s = decltype(S)::v; if constexpr (s < KS) gld16_sc1<(s & 3) * 1024>(kf[s], l16, kp + (s >> 2) * 4096); };
      auto rdq = [&](auto S) { constexpr int s = decltype(S)::v; if constexpr (s < KS) lds_128<s * 1024>(qf[s], lds0 + l16); };
      sfor<0, RA>(ldk);
      BAR();
      sfor<0, 4>(rdq);
      f32x16 acc;
      sfor<0, KS>([&](auto S) {
        constexpr int s = decltype(S)::v;
        WAIT_VM(imin(RA - 1, KS - 1 - s)); WAIT_LG(imin(3, KS - 1 - s));
        mfma_v<s == 0>(acc, kf[s], qf[s]);
        rdq(IC<s + 4>{}); ldk(IC<s + RA>{});
      });
      asm volatile("s_nop 15" : "+v"(acc));
      float m = acc[0];
#pragma unroll
      for (int r = 1; r < 16; ++r) m = fmaxf(m, acc[r]);
      m = xhalf_max(m);
      xm[wave * 32 + (lane & 31)] = wave >= own0 ? m : -3.0e38f;
      WAIT_LG(0);
      BAR();
#pragma unroll
      for (int w = 0; w < 5; ++w) m = w ? fmaxf(m, xm[w * 32 + (lane & 31)]) : xm[lane & 31];
      const float mc = -m * SM_C;
      float l = 0.f;
#pragma unroll
      for (int r = 0; r < 16; ++r) { const float pv = __builtin_amdgcn_exp2f(fmaf(acc[r], SM_C, mc)); acc[r] = pv; l += pv; }
      if (wave >= own0) {
        char* dst = ws + WS_P + (size_t)qt * PT_B + (size_t)kt * 2048 + l16;
        gst16(dst, pack8(acc, 0)); gst16(dst + 1024, pack8(acc, 1));
      }
      l = xhalf_sum(l);
      xm[160 + wave * 32 + (lane & 31)] = wave >= own0 ? l : 0.f;
      WAIT_LG(0);
      BAR();
      if (wave == 0) {
        l = 0.f;
#pragma unroll
        for (int w = 0; w < 5; ++w) l += xm[160 + w * 32 + (lane & 31)];
        __hip_atomic_store((GAS float*)(ws + WS_ST) + ((size_t)qt * NCH + sc) * 64 + lane, lane < 32 ? m : l, __ATOMIC_RELAXED, __HIP_MEMORY_SCOPE_AGENT);
      }
      WAIT_VM(0);
    }
    BAR();
    if (threadIdx.x == 0) flag_add(ws, F_P + b * 9 + 8);
    return;
  }
  if (__builtin_expect(wave >= 4, 1)) {
    if (wave >= 8) {
      if (wave == 8) flag_wait<FUSED>(ws, sel((lane & 2) || !isS, F_QKV + sel(pb < 8, b * 8 + pb, 16) * 3 + sel(isS, 0, 2),
                                              F_QKV + 1 + 3 * sel(lane & 1, sc < 6 ? b * 8 + ((kt0 + 4) >> 2) : 16, b * 8 + (kt0 >> 2))), sel((lane & 2) && isS, 5u, 6u));
      bars(NSTG + 1);
    } else {
      unsigned voff[5];
      w_voff(voff, lane, 1536);
      if (isS) {
#pragma unroll
        for (int i = 0; i < 5; ++i) voff[i] = i * TILE_B + l16 - (i < 4 ? i * 1024 : 0);
      }
      const unsigned stride = __builtin_amdgcn_readfirstlane(sel(isS, 2048, 32 * 1536)), lds = lds0 + (wave & 1) * 5120;
      const char* p = uni(sel(isS, (const char*)ws + WS_QKV + (unsigned)((NTILE + b * TPB + kt0) * TILE_B + (wave & 1) * 1024), A.wo_img + (n0 * 2 + (wave & 1) * 16 * 1536)));
      p = sadd(p, ((wave >> 1) & 1) * stride);
      asm volatile("s_nop 4" ::"s"(p), "s"(stride) : "memory");
      auto issue = [&](auto G) { glds5(voff, p, lds + (decltype(G)::v % P2_NS) * W_SLOT); p = sadd(p, 2 * stride); };
      if (isS) BAR();
      if (wave < 6) ring_loader2<NSTG, P2_NS, 5, 0>(issue, !isS); else ring_loader2<NSTG, P2_NS, 5, 1>(issue, !isS);
    }
  } else {
    f32x16 acc[5];
    const int tl = sel(pb < 8, pb * 4 + wave, 32);
    const bool active = pb < 8 || wave == 0;
    const char* priv = uni(ws + WS_QKV + (unsigned)((sel(isS, 0, 2 * NTILE) + b * TPB + tl) * TILE_B));
    asm volatile("s_nop 4" ::"s"(priv) : "memory");
    BAR();
    if (isS) {
      const int qt = b * TPB + tl;
      p2_consumer<2>(acc, priv, lds0, lane);
      p2_softmax<5>(acc, ws + WS_P + (size_t)qt * PT_B + (size_t)kt0 * 2048 + l16, (float*)(ws + WS_ST) + ((size_t)qt * NCH + sc) * 64, sc < 6 ? 0 : 2, lane);
    } else {
      p2_consumer<1>(acc, priv, lds0, lane);
      if (active) {
        char* dst = ws + WS_VP + ((size_t)b * 24 + (n0 >> 5)) * PT_B + (size_t)tl * 2048 + l16;
#pragma unroll
        for (int c = 0; c < 5; ++c) {
          if (sc == 4 && c == 0) continue;
          gst16(dst + (size_t)c * PT_B, pack8(acc[c], 0));
          gst16(dst + (size_t)c * PT_B + 1024, pack8(acc[c], 1));
        }
      }
    }
    WAIT_VM(0);
  }
  BAR();
  if (threadIdx.x == 0) flag_add(ws, sel(isS, F_P + b * 9 + pb, F_VP + b * 5 + sc));
}

constexpr int P3_RA = 11;
constexpr int NSTG3 = 22;
#define T_L3 6
#define T_L30 2
constexpr int ld3_cnt(int k) { return k == 0 ? T_L30 : imin(NSTG3, k + T_L3); }
DI void fma4(f32x16& acc, const f32x16& t, float f, int e) {
  asm volatile("v_fmac_f32 %0, %4, %8\n\tv_fmac_f32 %1, %5, %8\n\tv_fmac_f32 %2, %6, %8\n\tv_fmac_f32 %3, %7, %8"
               : "+v"(acc[e]), "+v"(acc[e + 1]), "+v"(acc[e + 2]), "+v"(acc[e + 3]) : "v"(t[e]), "v"(t[e + 1]), "v"(t[e + 2]), "v"(t[e + 3]), "v"(f));
}
DI void chunk_scales(float (&f)[NCH], const float (&mm)[NCH], const float (&ll)[NCH]) {
  float m = mm[0];
#pragma unroll
  for (int c = 1; c < NCH; ++c) m = fmaxf(m, mm[c]);
  float l = 0.f;
#pragma unroll
  for (int c = 0; c < NCH; ++c) { f[c] = __builtin_amdgcn_exp2f((mm[c] - m) * SM_C); l = fmaf(f[c], ll[c], l); }
  const float inv = 1.f / l;
#pragma unroll
  for (int c = 0; c < NCH; ++c) f[c] *= inv;
}
DI void ld_stats(float (&mm)[NCH], float (&ll)[NCH], const char* stp, int r) {
  sfor<0, NCH>([&](auto C) { constexpr int c = decltype(C)::v; gld4<c * 256>(mm[c], r * 4, stp); gld4<c * 256 + 128>(ll[c], r * 4, stp); });
}
DI void tie_stats(float (&mm)[NCH], float (&ll)[NCH]) {
#pragma unroll
  for (int c = 0; c < NCH; ++c) tie(mm[c], ll[c]);
}
template <bool FUSED> DI void phase3(const Args& A, unsigned task, char* smem, int wave, int lane) {
  char* ws = A.ws;
  const int b = (blockIdx.x >> 2) & 1, kind = (task >> 11) & 3;
  const int r = lane & 31, h = lane >> 5;
  const unsigned lds0 = (unsigned)(size_t)(LAS char*)smem;
  const unsigned l16 = lane * 16;
  if (kind == 0) return;
  if (kind == 1) {
    const int qb = (task >> 13) & 7, dp = (task >> 16) & 31;
    if (wave >= 7) return;
    if (wave == 6) {
      flag_wait<FUSED>(ws, sel(lane & 2, sel(lane & 1, F_GATE + dp, F_P + b * 9 + qb), F_VP + b * 5 + imin((dp * 64 + (lane & 1) * 63) / 160, 4)),
                       sel(lane & 2, sel(lane & 1, 1u, 7u), 9u));
      BAR();
      return;
    }
    if (__builtin_expect(wave >= 4, 1)) {
      const char* p = uni(ws + WS_VP + (unsigned)((b * 24 + dp * 2 + (wave - 4)) * PT_B));
      const unsigned lds = lds0 + (wave - 4) * (KSK * 1024);
      asm volatile("s_nop 4" ::"s"(p) : "memory");
      BAR();
      sfor<0, NSTG3>([&](auto K) {
        constexpr int k = decltype(K)::v;
        sfor<(k == 0 ? 0 : ld3_cnt(k - 1)), ld3_cnt(k)>([&](auto G) { constexpr int g = decltype(G)::v; gldsN<3>(l16, p + g * 3072, lds + g * 3072); });
        WAIT_VM(3 * (ld3_cnt(k) - k - 1)); BAR();
      });
      return;
    }
    const int qt = b * TPB + qb * 4 + wave;
    const char* pb = uni(ws + WS_P + (unsigned)(qt * PT_B));
    const char* stp = uni(ws + WS_ST + (unsigned)(qt * NCH * 256));
    asm volatile("s_nop 4" ::"s"(pb), "s"(stp) : "memory");
    BAR();
    u32x4 pf[KSK];
    auto ldp = [&](auto S) { constexpr int s = decltype(S)::v; if constexpr (s < KSK) gld16_sc1<(s & 3) * 1024>(pf[s], l16, pb + (s >> 2) * 4096); };
    float mm[NCH], ll[NCH], f[NCH];
    sfor<0, 3>(ldp);
    ld_stats(mm, ll, stp, r);
    sfor<3, P3_RA>(ldp);
    f32x16 acc[2], tmp[2];
    acc[0] = f32x16{}; acc[1] = f32x16{};
    tie(acc[0], acc[1]);
    u32x4 vf[2][2];
    unsigned rv0 = lds0 + l16, rv1 = rv0 + KSK * 1024;
    tie(rv0, rv1);
    auto rdv = [&](auto S, auto T) {
      constexpr int s = decltype(S)::v, t = decltype(T)::v;
      if constexpr (s < 64) lds_128<s * 1024>(vf[s & 1][t], t ? rv1 : rv0);
      else if constexpr (s < KSK) lds_128<(s - 2) * 1024>(vf[s & 1][t], (t ? rv1 : rv0) + 2048);
    };
    u32x4 xv[4];
    float gl;
    const unsigned orow = ((b * NI + (qb * 4 + wave) * 32 + (lane >> 3)) * D + dp * 64) * 2 + (lane & 7) * 16;
    BAR();
    rdv(IC<0>{}, IC<0>{}); rdv(IC<0>{}, IC<1>{}); rdv(IC<1>{}, IC<0>{}); rdv(IC<1>{}, IC<1>{});
    sfor<0, KSK>([&](auto S) {
      constexpr int s = decltype(S)::v, cur = s & 1, c = s / 10, cs = s % 10;
      if constexpr ((s + 2) % 3 == 0 && s + 2 < KSK) BAR();
      WAIT_VM(imin(P3_RA - 1, KSK - 1 - s) + (s < 3 ? 2 * NCH : 0) + (s > KSK - 1 - P3_RA ? 5 : 0));
      sfor<0, 2>([&](auto T) {
        constexpr int t = decltype(T)::v;
        WAIT_LG((1 - t) + (s + 1 < KSK ? 2 : 0) + (s + 2 < KSK ? t : 0));
        mfma_v<cs == 0>(tmp[t], vf[cur][t], pf[s]);
        rdv(IC<s + 2>{}, T);
        if constexpr (t == 0) {
          ldp(IC<s + P3_RA>{});
          if constexpr (s == KSK - 1 - P3_RA) {
            sfor<0, 4>([&](auto E) { constexpr int e = decltype(E)::v; gld16<0>(xv[e], orow, A.x_img + e * 8 * D * 2); });
            gld4<0>(gl, lane * 4, ws + WS_MOD + (unsigned)((b * 2304 + 1536 + dp * 64) * 4));
          }
        }
      });
      if constexpr (cs == 9) {
        asm volatile("s_nop 9" : "+v"(tmp[0]));
        fma4(acc[0], tmp[0], f[c], 0); fma4(acc[0], tmp[0], f[c], 4); fma4(acc[0], tmp[0], f[c], 8); fma4(acc[0], tmp[0], f[c], 12);
        fma4(acc[1], tmp[1], f[c], 0); fma4(acc[1], tmp[1], f[c], 4); fma4(acc[1], tmp[1], f[c], 8); fma4(acc[1], tmp[1], f[c], 12);
      }
      if constexpr (s == 4) { tie_stats(mm, ll); chunk_scales(f, mm, ll); SBAR(); }
    });
    asm volatile("s_nop 15" : "+v"(tmp[0]), "+v"(tmp[1]));
    WAIT_VM(0); tie(xv[0], xv[1], xv[2], xv[3], gl);
    BAR();
    {
      char* tb = smem + wave * 8192;
      *(float*)(smem + 32768 + wave * 256 + lane * 4) = gl;
#pragma unroll
      for (int e = 0; e < 8; ++e) {
        const int t = e >> 2, gq = e & 3;
        f32x4 y;
#pragma unroll
        for (int k = 0; k < 4; ++k) y[k] = fmaf(tmp[t][4 * gq + k], f[6], acc[t][4 * gq + k]);
        *(f32x4*)(tb + r * 256 + (((2 * e + h) ^ (r & 15)) * 16)) = y;
      }
      const f32x4 g0 = *(const f32x4*)(smem + 32768 + wave * 256 + (lane & 7) * 32), g1 = *(const f32x4*)(smem + 32768 + wave * 256 + (lane & 7) * 32 + 16);
#pragma unroll
      for (int e = 0; e < 4; ++e) {
        const int row = e * 8 + (lane >> 3), c2 = (lane & 7) * 2;
        const f32x4 y0 = *(const f32x4*)(tb + row * 256 + ((c2 ^ (row & 15)) * 16));
        const f32x4 y1 = *(const f32x4*)(tb + row * 256 + (((c2 + 1) ^ (row & 15)) * 16));
        u32x4 o = {pk_bf16(fmaf(g0[0], y0[0], bf_lo(xv[e][0])), fmaf(g0[1], y0[1], bf_hi(xv[e][0]))), pk_bf16(fmaf(g0[2], y0[2], bf_lo(xv[e][1])), fmaf(g0[3], y0[3], bf_hi(xv[e][1]))),
                   pk_bf16(fmaf(g1[0], y1[0], bf_lo(xv[e][2])), fmaf(g1[1], y1[1], bf_hi(xv[e][2]))), pk_bf16(fmaf(g1[2], y1[2], bf_lo(xv[e][3])), fmaf(g1[3], y1[3], bf_hi(xv[e][3])))};
        gst16(A.out + orow + (size_t)e * 8 * D * 2, o);
      }
    }
  } else {
    if (wave >= 8) return;
    const int T = (task >> 16) & 31;
    const int qt = b * TPB + 32;
    constexpr int LV = 0, LW = 8 * 10240, LR = LW + 768 * 64;
    if (wave == 7) {
      flag_wait<FUSED, 16>(ws, F_P + b * 9 + 8, 7u);
      flag_wait<FUSED>(ws, sel(lane < 9, F_QKV + 2 + 3 * sel(lane < 8, b * 8 + lane, 16), F_GATE + 12 + (T >> 1)), sel(lane < 9, 6u, 1u));
    }
    BAR();
    const int g0 = sel(wave < 6, wave * 5, 28);
    const char* vsrc = uni(ws + WS_QKV + (unsigned)((2 * NTILE + b * TPB + g0) * TILE_B + T * 2048));
    const char* pA = uni(ws + WS_P + (unsigned)(qt * PT_B + g0 * 2048));
    const char* stp = uni(ws + WS_ST + (unsigned)(qt * NCH * 256));
    const char* wsrc = uni(A.wo_txt + (unsigned)(96 * wave * 1536 + T * 64));
    asm volatile("s_nop 4" ::"s"(vsrc), "s"(pA), "s"(stp), "s"(wsrc) : "memory");
    const unsigned lv = lds0 + LV + wave * 10240;
    sfor<0, 5>([&](auto K) { constexpr int kt = decltype(K)::v; gldsN<2>(l16, vsrc + kt * TILE_B, lv + kt * 2048); });
    u32x4 pa[10];
    sfor<0, 10>([&](auto S) { constexpr int s = decltype(S)::v; gld16_sc1<(s & 3) * 1024>(pa[s], l16, pA + (s >> 2) * 4096); });
    float mm[NCH], ll[NCH], f[NCH];
    ld_stats(mm, ll, stp, r);
    {
      const unsigned wv = (lane >> 2) * 1536 + (lane & 3) * 16;
      sfor<0, 6>([&](auto I) { constexpr int i = decltype(I)::v; gldsN<1>(wv, wsrc + i * 16 * 1536, lds0 + LW + (96 * wave + 16 * i) * 64); });
    }
    const int blk = (lane >> 4) & 1, q4 = (lane & 15) >> 2, p = lane & 3;
    const unsigned va = lv + blk * 1024 + ((p & 1) * 32 + 4 * h + q4) * 16 + (p >> 1) * 8;
    WAIT_VM(6);
    tie_stats(mm, ll);
    tie(pa[0], pa[1], pa[2], pa[3], pa[4], pa[5], pa[6], pa[7], pa[8], pa[9]);
    f32x16 ta = f32x16{};
    {
      u32x4 v[10];
      auto rdv = [&](auto S) { constexpr int s = decltype(S)::v, o = (s >> 1) * 2048 + (s & 1) * 256; lds_tr2<o, o + 128>(v[s], va); };
      sfor<0, 7>(rdv);
      chunk_scales(f, mm, ll);
      const int skip = wave >= 6 ? 4 : 0;
      sfor<0, 10>([&](auto S) {
        constexpr int s = decltype(S)::v;
        WAIT_LG(2 * (imin(s + 7, 10) - s - 1)); tie(v[s]);
        if constexpr (s + 7 < 10) rdv(IC<s + 7>{});
        if (s >= skip) ta = mfma(v[s], pa[s], ta);
      });
    }
    float fa = f[0];
#pragma unroll
    for (int w = 1; w < 7; ++w) if (wave == w) fa = f[w];
    f32x4* red = (f32x4*)(smem + LR);
    if (wave < 7) {
#pragma unroll
      for (int e = 0; e < 4; ++e) red[(wave * 4 + e) * 64 + lane] = f32x4{ta[4 * e], ta[4 * e + 1], ta[4 * e + 2], ta[4 * e + 3]} * fa;
    }
    u32x2 xv; u32x4 gq;
    gld8<0>(xv, (r * D + 4 * h + (wave & 3) * 8) * 2, uni(A.x_txt + (unsigned)((b * NTX * D + T * 32) * 2)));
    gld16_sc1<0>(gq, h * 16 + (wave & 3) * 32, uni(ws + WS_MOD + (unsigned)(((MSPLIT * 2 + b) * 2304 + 1536 + T * 32) * 4)));
    WAIT_LG(0);
    BAR();
    if (wave < 2) {
      f32x4 s0 = red[(2 * wave) * 64 + lane], s1 = red[(2 * wave + 1) * 64 + lane];
#pragma unroll
      for (int w = 1; w < 7; ++w) { s0 += red[(w * 4 + 2 * wave) * 64 + lane]; s1 += red[(w * 4 + 2 * wave + 1) * 64 + lane]; }
      gst16(ws + WS_AT + (size_t)b * TILE_B + T * 2048 + wave * 1024 + l16, u32x4{pk_bf16(s0[0], s0[1]), pk_bf16(s0[2], s0[3]), pk_bf16(s1[0], s1[1]), pk_bf16(s1[2], s1[3])});
      WAIT_VM(0);
      if (lane == 0) flag_add(ws, F_TXT + b);
    }
    u32x4 wq[6];
    WAIT_VM(0); tie(xv, gq);
    {
      const unsigned wa = lds0 + LW + (96 * wave + 4 * h + q4) * 64 + blk * 32 + p * 8;
      sfor<0, 6>([&](auto S) { constexpr int s = decltype(S)::v; lds_tr2<s * 1024, s * 1024 + 512>(wq[s], wa); });
    }
    if (wave == 7) flag_wait<true>(ws, F_TXT + b, 48u);
    WAIT_LG(0);
    BAR();
    u32x4 af[6];
    const char* ab = uni(ws + WS_AT + (unsigned)(b * TILE_B + wave * 6144));
    asm volatile("s_nop 4" ::"s"(ab) : "memory");
    sfor<0, 6>([&](auto S) { constexpr int s = decltype(S)::v; gld16_sc1<(s & 3) * 1024>(af[s], l16, ab + (s >> 2) * 4096); });
    WAIT_VM(0);
    tie(af[0], af[1], af[2], af[3], af[4], af[5], wq[0], wq[1], wq[2], wq[3], wq[4], wq[5]);
    f32x16 tb = f32x16{};
#pragma unroll
    for (int s = 0; s < 6; ++s) tb = mfma(wq[s], af[s], tb);
#pragma unroll
    for (int e = 0; e < 4; ++e) red[(wave * 4 + e) * 64 + lane] = f32x4{tb[4 * e], tb[4 * e + 1], tb[4 * e + 2], tb[4 * e + 3]};
    WAIT_LG(0);
    BAR();
    if (wave < 4) {
      f32x4 o = red[wave * 64 + lane];
#pragma unroll
      for (int w = 1; w < 8; ++w) o += red[(w * 4 + wave) * 64 + lane];
      const f32x4 gt = __builtin_bit_cast(f32x4, gq);
      u32x2 ov = {pk_bf16(fmaf(gt[0], o[0], bf_lo(xv[0])), fmaf(gt[1], o[1], bf_hi(xv[0]))), pk_bf16(fmaf(gt[2], o[2], bf_lo(xv[1])), fmaf(gt[3], o[3], bf_hi(xv[1])))};
      *(u32x2*)(A.out + ((size_t)2 * NI * D + ((size_t)b * NTX + r) * D + T * 32 + 4 * h + wave * 8) * 2) = ov;
    }
  }
}

template <int PH> __global__ __launch_bounds__(768, 3) void k_main(Args A) {
  extern __shared__ __attribute__((aligned(16))) char smem[];
  const int wave = __builtin_amdgcn_readfirstlane(threadIdx.x >> 6), lane = threadIdx.x & 63;
  const unsigned task = A.task[blockIdx.x];
  const char* code;
  asm volatile("s_getpc_b64 %0" : "=s"(code));
  if constexpr (PH & 1) phase1(A, code, smem, wave, lane);
  if constexpr (PH & 2) phase2<PH == 7>(A, task, smem, wave, lane);
  if constexpr (PH & 4) phase3<PH == 7>(A, task, smem, wave, lane);
}

#define ONE_LAUNCH 1
extern "C" void kernel_launch(void* const* d_in, const int* in_sizes, int n_in, void* d_out, int out_size, void* d_ws, size_t ws_size,
                              hipStream_t stream) {
  (void)in_sizes; (void)n_in; (void)out_size; (void)ws_size;
  Args A;
  A.x_img = (const char*)d_in[0]; A.x_txt = (const char*)d_in[1]; A.cvec = (const unsigned short*)d_in[2];
  A.wqkv_img = (const char*)d_in[3]; A.wqkv_txt = (const char*)d_in[4]; A.wo_img = (const char*)d_in[5]; A.wo_txt = (const char*)d_in[6];
  A.wmod_img = (const unsigned short*)d_in[7]; A.wmod_txt = (const unsigned short*)d_in[8];
  A.ws = (char*)d_ws; A.out = (char*)d_out;
  int gate = 0;
  for (int x = 0; x < 8; ++x) {
    const int sub = x & 3, qh = sub & 1, ch = sub >> 1;
    int img = 0, ntx = 0;
    for (int i = 0; i < 32; ++i) {
      unsigned t = 0; bool light = false;
      if (sub < 2) {
        if (i < 28) t = 1u | ((sub * 4 + (i & 3)) << 3) | ((i >> 2) << 7);
        else { light = true; const int sc = sub * 4 + i - 28; if (sc < 7) t = 3u | (8u << 3) | (sc << 7); }
      } else {
        const int v = (sub - 2) * 24 + i;
        if (i < 24 && v < 45) t = 2u | ((v / 5) << 3) | ((v % 5) << 7); else light = true;
      }
      if (light && t == 0 && gate < 12 * (x / 4 + 1)) { t = 4u | ((gate & 1) << 3) | ((gate >> 1) << 7); ++gate; }
      if (light && ntx < (sub < 2 ? 4 : 8)) { t |= (2u << 11) | ((sub < 2 ? sub * 4 + ntx : 8 + (sub - 2) * 8 + ntx) << 16); ++ntx; }
      else if (!light && img < 24) { t |= (1u << 11) | ((qh * 4 + (img & 3)) << 13) | ((ch * 6 + (img >> 2)) << 16); ++img; }
      else if (light && img < 24 && sub == 3) { t |= (1u << 11) | ((qh * 4 + (img & 3)) << 13) | ((ch * 6 + (img >> 2)) << 16); ++img; }
      A.task[i * 8 + x] = t;
    }
  }
  k0_prep<<<K0_GRID, 512, 0, stream>>>((const unsigned short*)A.x_img, (const unsigned short*)A.x_txt, A.cvec, A.wmod_img, A.wmod_txt, A.wqkv_img, A.wqkv_txt, A.ws);
  (void)hipFuncSetAttribute((const void*)k_main<7>, hipFuncAttributeMaxDynamicSharedMemorySize, LDS_MAIN);
  k_main<7><<<256, 768, LDS_MAIN, stream>>>(A);
}
```
